```python
import torch

def _softmax(x, axis=-1):
    return torch.softmax(x, dim=axis)
def _gelu(x):
    return torch.nn.functional.gelu(x, approximate="tanh")
def _silu(x):
    return torch.nn.functional.silu(x)
def _sigmoid(x):
    return torch.sigmoid(x)
def _layernorm(x, axis=-1, eps=1e-5):
    m = x.mean(dim=axis, keepdim=True)
    v = x.var(dim=axis, keepdim=True, unbiased=False)
    return (x - m) / torch.sqrt(v + eps)
def _scatter_add(tgt, idx, src, axis=0):
    return tgt.clone().index_add_(axis, idx, src)
def _segment_sum(src, seg, n):
    out = torch.zeros((n,) + src.shape[1:], dtype=src.dtype)
    return out.index_add_(0, seg, src)
def _pad(x, pw):
    flat = tuple(v for pair in reversed(pw) for v in pair)
    return torch.nn.functional.pad(x, flat)

kernel_name = 'recurrent_scan__sliding_block_cached__dense_activation_squared_relu__decode'

def setup_inputs():

    _w = (lambda a: (lambda t: t.to(torch.bfloat16) if t.is_floating_point() else t)(torch.as_tensor(a)))
    def _A_setup():
        B, Sq, D = 1, 1, 640
        Hq, Hkv, Sk, Dh = 32, 32, 32768, 192
        assert Hq % Hkv == 0, (Hq, Hkv)
        return {
            "x": _w((lambda *s: torch.randn(*s, dtype=torch.bfloat16) * 0.05)(B, Sq, D)),
            "k_cache": _w((lambda *s: torch.randn(*s, dtype=torch.bfloat16) * 0.8)(B, Hkv, Sk, Dh)),
            "v_cache": _w((lambda *s: torch.randn(*s, dtype=torch.bfloat16) * 0.2)(B, Hkv, Sk, Dh)),
            "wq": _w((lambda *s: torch.randn(*s, dtype=torch.bfloat16) * 0.8)(D, Hq * Dh) * (D ** -0.5)),
            "wo": _w((lambda *s: torch.randn(*s, dtype=torch.bfloat16) * 0.2)(Hq * Dh, D) * ((Hq * Dh) ** -0.5)),
        }
    _A_in = _A_setup()

    def _B_setup():
        B, S, D, F = 1, 1, 640, 1536
        return {
            "x": _w((lambda *s: torch.randn(*s, dtype=torch.bfloat16) * 0.05)(B, S, D)),
            "w1": _w((lambda *s: torch.randn(*s, dtype=torch.bfloat16) * 0.1)(D, F)),
            "b1": _w((lambda *s: torch.randn(*s, dtype=torch.bfloat16) * 0.1)(F)),
            "w2": _w((lambda *s: torch.randn(*s, dtype=torch.bfloat16) * 0.1)(F, D)),
            "b2": _w((lambda *s: torch.randn(*s, dtype=torch.bfloat16) * 0.1)(D)),
        }
    _B_in = _B_setup()

    return {
        "A_x": _A_in["x"],
        "A_k_cache": _A_in["k_cache"],
        "A_v_cache": _A_in["v_cache"],
        "A_wq": _A_in["wq"],
        "A_wo": _A_in["wo"],
        "B_x": _B_in["x"],
        "B_w1": _B_in["w1"],
        "B_b1": _B_in["b1"],
        "B_w2": _B_in["w2"],
        "B_b2": _B_in["b2"],
    }

def reference(A_x, A_k_cache, A_v_cache, A_wq, A_wo, B_x, B_w1, B_b1, B_w2, B_b2):
    def _A_ref(x, k_cache, v_cache, wq, wo):
        B, Sq, D = x.shape
        Hq, Dh = 32, 192
        Hkv, Sk = k_cache.shape[1], k_cache.shape[2]
        W = min(4096, max(256, Sk // 4))
        rep = Hq // Hkv
        q = (lambda x, *d: x.permute(*d))((x @ wq).reshape(B, Sq, Hq, Dh), 0, 2, 1, 3)
        k = (lambda x, n, axis: torch.repeat_interleave(x, n, dim=axis))(k_cache[:, :, Sk - W:, :], rep, axis=1)
        v = (lambda x, n, axis: torch.repeat_interleave(x, n, dim=axis))(v_cache[:, :, Sk - W:, :], rep, axis=1)
        scores = torch.einsum("bhqd,bhkd->bhqk", q, k) * (Dh ** -0.5)
        p = _softmax(scores, axis=-1)
        o = torch.einsum("bhqk,bhkd->bhqd", p, v)
        return (lambda x, *d: x.permute(*d))(o, 0, 2, 1, 3).reshape(B, Sq, Hq * Dh) @ wo

    def _B_ref(x, w1, b1, w2, b2):
        h = torch.einsum("bsd,df->bsf", x, w1) + b1
        zero = torch.zeros_like(h)
        r = torch.where(h > zero, h, zero)
        h = r * r
        out = torch.einsum("bsf,fd->bsd", h, w2) + b2
        return out

    x = A_x
    xn = _layernorm(x, axis=-1)
    s = _A_ref(xn, A_k_cache, A_v_cache, A_wq, A_wo)
    g = _silu(_B_ref(xn, B_w1, B_b1, B_w2, B_b2))
    return x + s * g
```

```cpp
#include <hip/hip_runtime.h>
#include <hip/hip_bf16.h>
#include <stdint.h>

typedef unsigned v4u __attribute__((ext_vector_type(4)));
typedef float f32x4 __attribute__((ext_vector_type(4)));
typedef short bf16x8 __attribute__((ext_vector_type(8)));
typedef __bf16 bf16x2_t __attribute__((ext_vector_type(2)));
typedef unsigned long long u64;
#define GAS __attribute__((address_space(1)))
#define LAS __attribute__((address_space(3)))
#define RLX_AGENT __ATOMIC_RELAXED, __HIP_MEMORY_SCOPE_AGENT
#define RLX_WG __ATOMIC_RELAXED, __HIP_MEMORY_SCOPE_WORKGROUP
typedef GAS u64 gu64;

#define WIN 2
#define KV_AUX 2
#define W_AUX 2

constexpr int DM = 640, HQ = 32, DH = 192, SK = 32768, WN = 4096, FF = 1536;
constexpr int NWG = 256, NT = 1024;
constexpr int ROWB = DH * 2;
constexpr int KEYS_WG = WN / 8;
constexpr unsigned OOB = 0x40000000u;
constexpr int RSRC_W3 = 0x00020000;
constexpr unsigned SPIN_CAP = 1u << 18;

constexpr size_t G_EPOCH = 0;
constexpr size_t G_Q = 64;
constexpr size_t G_O = G_Q + (size_t)NWG * 256;
constexpr size_t G_Y = G_O + (size_t)NWG * 256;
constexpr size_t G_Z = G_Y + (size_t)8 * 20 * 32 * 4;
constexpr size_t G_END = G_Z + (size_t)8 * 20 * 32 * 4;
constexpr size_t G_TS = G_END + 64;

constexpr int QP_STR = 260;
constexpr int OP_STR = 196;
constexpr int YP_STR = 84;
constexpr int L_QP = 0;
constexpr int L_OP = 5200;
constexpr int L_YP = L_OP + 32 * OP_STR;
constexpr int L_QB = L_YP + 16 * YP_STR;
constexpr int L_OB = L_QB + 96;
constexpr int L_HB = L_OB + 96;
constexpr int L_M = L_HB + 48;
constexpr int L_L = L_M + 16;
constexpr int L_FLAG = L_L + 16;
constexpr int L_END = L_FLAG + 4;

struct Params {
  const unsigned short* x;
  const unsigned short* k;
  const unsigned short* v;
  const unsigned short* wq;
  const unsigned short* wo;
  const unsigned short* w1;
  const unsigned short* b1;
  const unsigned short* w2;
  const unsigned short* b2;
  unsigned short* out;
  u64* ws;
};

__device__ __forceinline__ float bf2f(unsigned h) { return __builtin_bit_cast(float, h << 16); }
__device__ __forceinline__ unsigned cvtpk(float lo, float hi) {
  bf16x2_t r = {(__bf16)lo, (__bf16)hi};
  return __builtin_bit_cast(unsigned, r);
}
__device__ __forceinline__ float rbf(float f) { return bf2f(cvtpk(f, f) & 0xffffu); }
__device__ __forceinline__ float dot2(unsigned a, unsigned b, float acc) {
  return __builtin_amdgcn_fdot2_f32_bf16(__builtin_bit_cast(bf16x2_t, a), __builtin_bit_cast(bf16x2_t, b), acc, false);
}
template <int CTRL> __device__ __forceinline__ float dpp(float x) {
  return __builtin_bit_cast(float, __builtin_amdgcn_mov_dpp(__builtin_bit_cast(int, x), CTRL, 0xf, 0xf, true));
}
constexpr int XOR1 = 0xB1, XOR2 = 0x4E, HMIR = 0x141, ROR8 = 0x128;
__device__ __forceinline__ float sum8(float x) { x += dpp<XOR1>(x); x += dpp<XOR2>(x); x += dpp<HMIR>(x); return x; }
__device__ __forceinline__ float sum16(float x) { x = sum8(x); x += dpp<ROR8>(x); return x; }
__device__ __forceinline__ float max16(float x) {
  x = fmaxf(x, dpp<XOR1>(x)); x = fmaxf(x, dpp<XOR2>(x)); x = fmaxf(x, dpp<HMIR>(x)); x = fmaxf(x, dpp<ROR8>(x)); return x; }
__device__ __forceinline__ float xrow_sum(float x) {
  auto s = __builtin_amdgcn_permlane16_swap(__float_as_uint(x), __float_as_uint(x), false, false);
  x = __uint_as_float(s[0]) + __uint_as_float(s[1]);
  auto t = __builtin_amdgcn_permlane32_swap(__float_as_uint(x), __float_as_uint(x), false, false);
  return __uint_as_float(t[0]) + __uint_as_float(t[1]);
}
__device__ __forceinline__ float xrow_max(float x) {
  auto s = __builtin_amdgcn_permlane16_swap(__float_as_uint(x), __float_as_uint(x), false, false);
  x = fmaxf(__uint_as_float(s[0]), __uint_as_float(s[1]));
  auto t = __builtin_amdgcn_permlane32_swap(__float_as_uint(x), __float_as_uint(x), false, false);
  return fmaxf(__uint_as_float(t[0]), __uint_as_float(t[1]));
}
__device__ __forceinline__ f32x4 mfma(v4u a, v4u b, f32x4 c) {
  return __builtin_amdgcn_mfma_f32_16x16x32_bf16(__builtin_bit_cast(bf16x8, a), __builtin_bit_cast(bf16x8, b), c, 0, 0, 0);
}
__device__ __forceinline__ __amdgpu_buffer_rsrc_t mkrsrc(const void* p, unsigned bytes) {
  return __builtin_amdgcn_make_buffer_rsrc((void*)p, 0, bytes, RSRC_W3);
}
#define SB() __builtin_amdgcn_sched_barrier(0)
#define VMWAIT(n) asm volatile("s_waitcnt vmcnt(%0)" ::"n"(n) : "memory")
#define LGKM0() asm volatile("s_waitcnt lgkmcnt(0)" ::: "memory")
#define BAR() do { LGKM0(); __builtin_amdgcn_s_barrier(); asm volatile("" ::: "memory"); } while (0)

__device__ __forceinline__ void put_gran(gu64* g, float v, unsigned tlo, unsigned thi) {
  const unsigned hi = (__float_as_uint(v) & 0xffffff00u) | thi;
  __hip_atomic_store(g, ((u64)hi << 32) | tlo, RLX_AGENT);
}
__device__ __forceinline__ bool gran_ok(u64 g, unsigned tlo, unsigned thi) {
  return ((unsigned)g == tlo) & (((unsigned)(g >> 32) & 0xffu) == thi);
}
__device__ __forceinline__ float gran_val(u64 g) { return __uint_as_float((unsigned)(g >> 32) & 0xffffff00u); }

__device__ __forceinline__ void lds_wait_ge(LAS unsigned* f, unsigned n) {
  while (__hip_atomic_load(f, RLX_WG) < n) __builtin_amdgcn_s_sleep(1);
  asm volatile("" ::: "memory");
}
__device__ __forceinline__ void lds_signal(LAS unsigned* f, int lane) {
  LGKM0();
  if (lane == 0) (void)__hip_atomic_fetch_add(f, 1u, RLX_WG);
}

#define TS(i) do {} while (0)

__global__ void __launch_bounds__(NT) fused_decode(Params p) {
  __shared__ __attribute__((aligned(16))) float lds_[L_END];
  LAS float* lds = (LAS float*)lds_;
  LAS unsigned* flags = (LAS unsigned*)(lds + L_FLAG);
  const int tid = threadIdx.x, l = tid & 63, w = __builtin_amdgcn_readfirstlane(tid >> 6);
  const int b = blockIdx.x;
  const int s = b & 7, h = b >> 3;
  const int h2 = (b & 7) * 4 + (b >> 6), s2 = (b >> 3) & 7;
  TS(0);
  gu64* ws = (gu64*)p.ws;

  const unsigned voff = (unsigned)((l >> 3) * ROWB + (l & 7) * 16);
  const size_t kvoff = ((size_t)h * SK + (SK - WN) + (size_t)s * KEYS_WG) * DH;
  const __amdgpu_buffer_rsrc_t rk = mkrsrc(p.k + kvoff, KEYS_WG * ROWB);
  const __amdgpu_buffer_rsrc_t rv = mkrsrc(p.v + kvoff, KEYS_WG * ROWB);
  const int woff = w * 4 * 8 * ROWB;

  v4u mk;
  {
    const unsigned hm = (l & 1) ? 0xffff0000u : 0x0000ffffu;
    const int q4 = (l & 7) >> 1;
    mk.x = q4 == 0 ? hm : 0u; mk.y = q4 == 1 ? hm : 0u; mk.z = q4 == 2 ? hm : 0u; mk.w = q4 == 3 ? hm : 0u;
  }

  v4u K[12], V[12];
  u64 ep;

  if (w < 10) {
    const int row0 = 80 * s + 8 * w;
    const __amdgpu_buffer_rsrc_t rq = mkrsrc(p.wq + (size_t)row0 * (HQ * DH) + h * DH, 8 * HQ * DH * 2);
    const __amdgpu_buffer_rsrc_t r1 = mkrsrc(p.w1 + (size_t)row0 * FF + h * 48, 8 * FF * 2);
    const unsigned qoff = (unsigned)((l >> 3) * (HQ * DH * 2) + (l & 7) * 16);
    const unsigned w1off = (l & 7) < 6 ? (unsigned)((l >> 3) * (FF * 2) + (l & 7) * 16) : OOB;
    v4u A[4];
#pragma unroll
    for (int j = 0; j < 3; ++j) A[j] = __builtin_amdgcn_raw_buffer_load_b128(rq, qoff + j * 128, 0, W_AUX);
    A[3] = __builtin_amdgcn_raw_buffer_load_b128(r1, w1off, 0, W_AUX);
    const GAS unsigned* x32 = (const GAS unsigned*)p.x;
    unsigned xd[5];
#pragma unroll
    for (int i = 0; i < 5; ++i) xd[i] = x32[l + 64 * i];
    const unsigned xr = ((const GAS unsigned short*)p.x)[row0 + (l >> 3)];
    ep = __hip_atomic_load(ws + G_EPOCH, RLX_AGENT);
    SB();
#pragma unroll
    for (int i = 0; i < WIN; ++i) K[i] = __builtin_amdgcn_raw_buffer_load_b128(rk, voff + (i % 3) * 128, woff + (i / 3) * 8 * ROWB, KV_AUX);
    SB();
    if (tid == 0) { flags[0] = 0; flags[1] = 0; flags[2] = 0; flags[3] = 0; }
    BAR();
    float xs[10], sum = 0.f;
#pragma unroll
    for (int i = 0; i < 5; ++i) { xs[2 * i] = bf2f(xd[i] & 0xffffu); xs[2 * i + 1] = __uint_as_float(xd[i] & 0xffff0000u); sum += xs[2 * i] + xs[2 * i + 1]; }
    sum = xrow_sum(sum16(sum));
    const float mean = sum * (1.0f / DM);
    float sq = 0.f;
#pragma unroll
    for (int i = 0; i < 10; ++i) { const float d = xs[i] - mean; sq = fmaf(d, d, sq); }
    sq = xrow_sum(sum16(sq));
    const float sd = rbf(sqrtf(rbf(rbf(sq * (1.0f / DM)) + 1e-5f)));
    const float xn = rbf(bf2f(xr) - rbf(mean)) / sd;
    const unsigned xdup = cvtpk(xn, xn);
    const v4u sel = mk & xdup;
    const bool valid = ((l >> 3) & 1) == (l >> 5);
    LAS float* qp = lds + L_QP + (2 * w + (l >> 5)) * QP_STR + 8 * (l & 7) + 4 * ((l >> 4) & 1);
#pragma unroll
    for (int j = 0; j < 4; ++j) {
      const f32x4 d = mfma(sel, A[j], f32x4{0.f, 0.f, 0.f, 0.f});
      if (valid) *(LAS f32x4*)(qp + 64 * j) = d;
    }
    lds_signal(flags + 0, l);
    TS(1);
#pragma unroll
    for (int i = WIN; i < 12; ++i) {
      VMWAIT(WIN - 1);
      K[i] = __builtin_amdgcn_raw_buffer_load_b128(rk, voff + (i % 3) * 128, woff + (i / 3) * 8 * ROWB, KV_AUX);
      SB();
    }
  } else if (w < 12) {
#pragma unroll
    for (int i = 0; i < WIN; ++i) K[i] = __builtin_amdgcn_raw_buffer_load_b128(rk, voff + (i % 3) * 128, woff + (i / 3) * 8 * ROWB, KV_AUX);
    ep = __hip_atomic_load(ws + G_EPOCH, RLX_AGENT);
    SB();
    BAR();
#pragma unroll
    for (int i = WIN; i < 12; ++i) {
      VMWAIT(WIN - 1);
      K[i] = __builtin_amdgcn_raw_buffer_load_b128(rk, voff + (i % 3) * 128, woff + (i / 3) * 8 * ROWB, KV_AUX);
      SB();
    }
  } else {
    const int c = (w - 12) * 60 + l;
    const bool act = l < 60;
    K[0] = __builtin_amdgcn_raw_buffer_load_b128(rk, voff, woff, KV_AUX);
    ep = __hip_atomic_load(ws + G_EPOCH, RLX_AGENT);
#pragma unroll
    for (int i = 1; i < 12; ++i) K[i] = __builtin_amdgcn_raw_buffer_load_b128(rk, voff + (i % 3) * 128, woff + (i / 3) * 8 * ROWB, KV_AUX);
    SB();
    BAR();
    const u64 tag = ep + 1;
    const unsigned tlo = (unsigned)tag, thi = (unsigned)(tag >> 32) & 0xffu;
    lds_wait_ge(flags + 0, 10);
    TS(1);
    float acc = 0.f;
    const int cc = act ? c : 0;
#pragma unroll
    for (int i = 0; i < 20; ++i) acc += lds[L_QP + i * QP_STR + cc];
    if (act) put_gran(ws + G_Q + (size_t)(h * 8 + s) * 256 + c, acc, tlo, thi);
    TS(2);
    gu64* src = ws + G_Q + (size_t)((c < 192 ? h : h2) * 8) * 256 + cc;
    float tot;
    for (unsigned spins = 0;; ++spins) {
      u64 g[8];
#pragma unroll
      for (int i = 0; i < 8; ++i) g[i] = __hip_atomic_load(src + i * 256, RLX_AGENT);
      bool ok = true;
      tot = 0.f;
#pragma unroll
      for (int i = 0; i < 8; ++i) { ok &= gran_ok(g[i], tlo, thi); tot += gran_val(g[i]); }
      if (__all(ok | !act) || spins > SPIN_CAP) break;
      __builtin_amdgcn_s_sleep(2);
    }
    TS(3);
    if (act && c < 192) ((LAS unsigned short*)(lds + L_QB))[c] = (unsigned short)(cvtpk(tot, tot) & 0xffffu);
    lds_signal(flags + 1, l);
    if (w == 15) {
      v4u W2[12];
      const unsigned b1v = ((const GAS unsigned short*)p.b1)[h2 * 48 + (l >= 12 && act ? l - 12 : 0)];
      const __amdgpu_buffer_rsrc_t r2 = mkrsrc(p.w2 + (size_t)(h2 * 48) * DM + s2 * 80, 48 * DM * 2);
      const unsigned o2 = (l & 15) < 10 ? (unsigned)((l >> 4) * (DM * 2) + (l & 15) * 16) : OOB;
#pragma unroll
      for (int i = 0; i < 12; ++i) W2[i] = __builtin_amdgcn_raw_buffer_load_b128(r2, o2, i * 4 * DM * 2, W_AUX);
      SB();
      float hv = rbf(rbf(tot) + bf2f(b1v));
      hv = hv > 0.f ? hv : 0.f;
      hv = hv * hv;
      if (l >= 12 && act) ((LAS unsigned*)(lds + L_HB))[l - 12] = cvtpk(hv, hv);
      LGKM0();
      f32x4 z = {0.f, 0.f, 0.f, 0.f};
      const unsigned lm = (l & 8) ? 0u : 0xffffffffu;
#pragma unroll
      for (int i = 0; i < 12; ++i) {
        const unsigned hd = ((LAS unsigned*)(lds + L_HB))[4 * i + (l >> 4)] & lm;
        z = mfma(mk & hd, W2[i], z);
      }
      if ((l & 15) < 10 && l < 32) {
        gu64* dst = ws + G_Z + ((size_t)(s2 * 20 + 2 * (l & 15) + (l >> 4)) * 32 + h2) * 4;
#pragma unroll
        for (int r = 0; r < 4; ++r) put_gran(dst + r, z[r], tlo, thi);
      }
    }
  }
  const u64 tag = ep + 1;
  const unsigned tlo = (unsigned)tag, thi = (unsigned)(tag >> 32) & 0xffu;

  float sc[4];
  v4u qf[3];
#pragma unroll
  for (int g = 0; g < 4; ++g) {
#pragma unroll
    for (int j = 0; j < 3; ++j) {
      VMWAIT(WIN - 1);
      V[g * 3 + j] = __builtin_amdgcn_raw_buffer_load_b128(rv, voff + j * 128, woff + g * 8 * ROWB, KV_AUX);
      SB();
    }
    if (g == 0) {
      TS(4);
      lds_wait_ge(flags + 1, 4);
#pragma unroll
      for (int j = 0; j < 3; ++j) qf[j] = *(LAS v4u*)(lds + L_QB + 32 * j + 4 * (l & 7));
      TS(5);
    }
    float d0 = 0.f, d1 = 0.f;
#pragma unroll
    for (int j = 0; j < 3; ++j) {
      d0 = dot2(K[g * 3 + j].x, qf[j].x, d0); d1 = dot2(K[g * 3 + j].y, qf[j].y, d1);
      d0 = dot2(K[g * 3 + j].z, qf[j].z, d0); d1 = dot2(K[g * 3 + j].w, qf[j].w, d1);
    }
    sc[g] = rbf(rbf(sum8(d0 + d1)) * 0.07216878364870322f);
    SB();
  }
  v4u WO[3];
  {
    const __amdgpu_buffer_rsrc_t ro = mkrsrc(p.wo + (size_t)(h2 * DH + 12 * w) * DM + s2 * 80, 12 * DM * 2);
    const unsigned oo = (l & 15) < 10 ? (unsigned)((l >> 4) * (DM * 2) + (l & 15) * 16) : OOB;
#pragma unroll
    for (int i = 0; i < 3; ++i) { VMWAIT(WIN - 1); WO[i] = __builtin_amdgcn_raw_buffer_load_b128(ro, oo, i * 4 * DM * 2, W_AUX); SB(); }
  }
  unsigned xfin = 0, b2fin = 0;
  if (w == 5 && h2 < 20) {
    xfin = ((const GAS unsigned*)p.x)[s2 * 40 + h2 * 2 + (l & 1)];
    b2fin = ((const GAS unsigned*)p.b2)[s2 * 40 + h2 * 2 + (l & 1)];
  }
  float mw = fmaxf(fmaxf(sc[0], sc[1]), fmaxf(sc[2], sc[3]));
  mw = fmaxf(mw, dpp<ROR8>(mw));
  mw = xrow_max(mw);
  if (l == 0) lds[L_M + w] = mw;
  lds_signal(flags + 2, l);
  constexpr float LOG2E = 1.4426950408889634f;
  float pr[4], lw = 0.f;
#pragma unroll
  for (int g = 0; g < 4; ++g) { pr[g] = __builtin_amdgcn_exp2f((sc[g] - mw) * LOG2E); lw += pr[g]; }
  lw += dpp<ROR8>(lw);
  lw = xrow_sum(lw);
  TS(6);
  f32x4 acc[3] = {{0.f, 0.f, 0.f, 0.f}, {0.f, 0.f, 0.f, 0.f}, {0.f, 0.f, 0.f, 0.f}};
#pragma unroll
  for (int g = 0; g < 4; ++g) {
    const v4u sel = mk & cvtpk(pr[g], pr[g]);
#pragma unroll
    for (int j = 0; j < 3; ++j) acc[j] = mfma(sel, V[g * 3 + j], acc[j]);
  }
  lds_wait_ge(flags + 2, 16);
  const float mwg = max16(lds[L_M + (l & 15)]);
  const float fw = __builtin_amdgcn_exp2f((mw - mwg) * LOG2E);
  {
    const bool valid = ((l >> 3) & 1) == (l >> 5);
    LAS float* op = lds + L_OP + (w + 16 * (l >> 5)) * OP_STR + 8 * (l & 7) + 4 * ((l >> 4) & 1);
#pragma unroll
    for (int j = 0; j < 3; ++j) if (valid) *(LAS f32x4*)(op + 64 * j) = acc[j] * fw;
    if (l == 0) lds[L_L + w] = lw * fw;
  }
  TS(7);
  BAR();
  if (w < 12) {
    const int c4 = tid >> 4, pg = tid & 15;
    const f32x4 a = *(LAS f32x4*)(lds + L_OP + pg * OP_STR + 4 * c4);
    const f32x4 bq = *(LAS f32x4*)(lds + L_OP + (pg + 16) * OP_STR + 4 * c4);
    f32x4 t = a + bq;
#pragma unroll
    for (int r = 0; r < 4; ++r) t[r] = sum16(t[r]);
    const float vsel = pg == 0 ? t[0] : pg == 1 ? t[1] : pg == 2 ? t[2] : t[3];
    if (pg < 4) put_gran(ws + G_O + (size_t)(h * 8 + s) * 256 + 4 * c4 + pg, vsel, tlo, thi);
  } else if (w == 12) {
    const float lsum = sum16(lds[L_L + (l & 15)]);
    if (l < 2) put_gran(ws + G_O + (size_t)(h * 8 + s) * 256 + 192 + l, l == 0 ? mwg : lsum, tlo, thi);
  }
  TS(8);
  if (w >= 13) {
    const int d = (w - 13) * 64 + l;
    gu64* src = ws + G_O + (size_t)(h2 * 8) * 256;
    u64 g[8], gm;
    for (unsigned spins = 0;; ++spins) {
#pragma unroll
      for (int i = 0; i < 8; ++i) g[i] = __hip_atomic_load(src + i * 256 + d, RLX_AGENT);
      gm = __hip_atomic_load(src + ((l >> 1) & 7) * 256 + 192 + (l & 1), RLX_AGENT);
      bool ok = gran_ok(gm, tlo, thi);
#pragma unroll
      for (int i = 0; i < 8; ++i) ok &= gran_ok(g[i], tlo, thi);
      if (__all(ok) || spins > SPIN_CAP) break;
      __builtin_amdgcn_s_sleep(1);
    }
    const float ml = gran_val(gm);
    const float mo = dpp<XOR1>(ml);
    const float ms = (l & 1) ? mo : ml, ls = (l & 1) ? ml : mo;
    const float M = max16(ms);
    const float e = __builtin_amdgcn_exp2f((ms - M) * LOG2E);
    const float L = 0.5f * sum16(e * ls);
    const float f = e / L;
    float o = 0.f;
#pragma unroll
    for (int i = 0; i < 8; ++i) o = fmaf(__builtin_bit_cast(float, __builtin_amdgcn_readlane(__builtin_bit_cast(int, f), 2 * i)), gran_val(g[i]), o);
    ((LAS unsigned short*)(lds + L_OB))[d] = (unsigned short)(cvtpk(o, o) & 0xffffu);
  }
  TS(9);
  BAR();
  {
    f32x4 y = {0.f, 0.f, 0.f, 0.f};
    const unsigned lm = (l & 8) ? 0u : 0xffffffffu;
#pragma unroll
    for (int i = 0; i < 3; ++i) {
      const unsigned ov = ((LAS unsigned short*)(lds + L_OB))[12 * w + 4 * i + (l >> 4)];
      y = mfma(mk & ((ov | (ov << 16)) & lm), WO[i], y);
    }
    if ((l & 15) < 10 && l < 32) *(LAS f32x4*)(lds + L_YP + w * YP_STR + 8 * (l & 15) + 4 * (l >> 4)) = y;
  }
  BAR();
  if (w < 5) {
    const int c4 = tid >> 4, pg = tid & 15;
    f32x4 t = *(LAS f32x4*)(lds + L_YP + pg * YP_STR + 4 * c4);
#pragma unroll
    for (int r = 0; r < 4; ++r) t[r] = sum16(t[r]);
    const float vsel = pg == 0 ? t[0] : pg == 1 ? t[1] : pg == 2 ? t[2] : t[3];
    if (pg < 4) put_gran(ws + G_Y + ((size_t)(s2 * 20 + c4) * 32 + h2) * 4 + pg, vsel, tlo, thi);
    TS(10);
  } else if (w == 5 && h2 < 20) {
    gu64* ys = ws + G_Y + (size_t)(s2 * 20 + h2) * 128 + 2 * l;
    gu64* zs = ws + G_Z + (size_t)(s2 * 20 + h2) * 128 + 2 * l;
    u64 y0, y1, z0, z1;
    for (unsigned spins = 0;; ++spins) {
      y0 = __hip_atomic_load(ys, RLX_AGENT); y1 = __hip_atomic_load(ys + 1, RLX_AGENT);
      z0 = __hip_atomic_load(zs, RLX_AGENT); z1 = __hip_atomic_load(zs + 1, RLX_AGENT);
      const bool ok = gran_ok(y0, tlo, thi) & gran_ok(y1, tlo, thi) & gran_ok(z0, tlo, thi) & gran_ok(z1, tlo, thi);
      if (__all(ok) || spins > SPIN_CAP) break;
      __builtin_amdgcn_s_sleep(1);
    }
    float v4[4] = {gran_val(y0), gran_val(y1), gran_val(z0), gran_val(z1)};
#pragma unroll
    for (int r = 0; r < 4; ++r) {
      float t = v4[r];
      t += dpp<XOR2>(t);
      const float u = dpp<HMIR>(t);
      t += dpp<XOR1>(u);
      t += dpp<ROR8>(t);
      v4[r] = xrow_sum(t);
    }
    float res[2];
#pragma unroll
    for (int r = 0; r < 2; ++r) {
      const float sv = rbf(v4[r]);
      const float bb = r == 0 ? bf2f(b2fin & 0xffffu) : __uint_as_float(b2fin & 0xffff0000u);
      const float xx = r == 0 ? bf2f(xfin & 0xffffu) : __uint_as_float(xfin & 0xffff0000u);
      const float o2 = rbf(rbf(v4[2 + r]) + bb);
      const float gt = rbf(o2 / (1.0f + __expf(-o2)));
      res[r] = xx + rbf(sv * gt);
    }
    if (l < 2) ((GAS unsigned*)p.out)[s2 * 40 + h2 * 2 + l] = cvtpk(res[0], res[1]);
    if (b == 0 && l == 0) __hip_atomic_store(ws + G_EPOCH, tag, RLX_AGENT);
    TS(11);
  }
}

extern "C" void kernel_launch(void* const* d_in, const int* in_sizes, int n_in,
                              void* d_out, int out_size, void* d_ws, size_t ws_size,
                              hipStream_t stream) {
  Params p;
  p.x = (const unsigned short*)d_in[0];
  p.k = (const unsigned short*)d_in[1];
  p.v = (const unsigned short*)d_in[2];
  p.wq = (const unsigned short*)d_in[3];
  p.wo = (const unsigned short*)d_in[4];
  p.w1 = (const unsigned short*)d_in[6];
  p.b1 = (const unsigned short*)d_in[7];
  p.w2 = (const unsigned short*)d_in[8];
  p.b2 = (const unsigned short*)d_in[9];
  p.out = (unsigned short*)d_out;
  p.ws = (u64*)d_ws;
  hipLaunchKernelGGL(fused_decode, dim3(NWG), dim3(NT), 0, stream, p);
}
```

```cpp
#define PROBE 0
#include <hip/hip_runtime.h>
#include <hip/hip_bf16.h>
#include <stdint.h>

typedef unsigned v4u __attribute__((ext_vector_type(4)));
typedef float f32x4 __attribute__((ext_vector_type(4)));
typedef short bf16x8 __attribute__((ext_vector_type(8)));
typedef __bf16 bf16x2_t __attribute__((ext_vector_type(2)));
typedef unsigned long long u64;
#define GAS __attribute__((address_space(1)))
#define LAS __attribute__((address_space(3)))
#define RLX_AGENT __ATOMIC_RELAXED, __HIP_MEMORY_SCOPE_AGENT
#define RLX_WG __ATOMIC_RELAXED, __HIP_MEMORY_SCOPE_WORKGROUP
typedef GAS u64 gu64;

#define WIN 2
#define KV_AUX 2
#define W_AUX 2

constexpr int DM = 640, HQ = 32, DH = 192, SK = 32768, WN = 4096, FF = 1536;
constexpr int NWG = 256, NT = 1024;
constexpr int ROWB = DH * 2;
constexpr int KEYS_WG = WN / 8;
constexpr unsigned OOB = 0x40000000u;
constexpr int RSRC_W3 = 0x00020000;
constexpr unsigned SPIN_CAP = 1u << 18;

constexpr size_t G_EPOCH = 0;
constexpr size_t G_Q = 64;
constexpr size_t G_O = G_Q + (size_t)NWG * 256;
constexpr size_t G_Y = G_O + (size_t)NWG * 256;
constexpr size_t G_Z = G_Y + (size_t)8 * 20 * 32 * 4;
constexpr size_t G_END = G_Z + (size_t)8 * 20 * 32 * 4;
constexpr size_t G_TS = G_END + 64;

constexpr int QP_STR = 260;
constexpr int OP_STR = 196;
constexpr int YP_STR = 84;
constexpr int L_QP = 0;
constexpr int L_OP = 5200;
constexpr int L_YP = L_OP + 32 * OP_STR;
constexpr int L_QB = L_YP + 16 * YP_STR;
constexpr int L_OB = L_QB + 96;
constexpr int L_HB = L_OB + 96;
constexpr int L_M = L_HB + 48;
constexpr int L_L = L_M + 16;
constexpr int L_FLAG = L_L + 16;
constexpr int L_END = L_FLAG + 4;

struct Params {
  const unsigned short* x;
  const unsigned short* k;
  const unsigned short* v;
  const unsigned short* wq;
  const unsigned short* wo;
  const unsigned short* w1;
  const unsigned short* b1;
  const unsigned short* w2;
  const unsigned short* b2;
  unsigned short* out;
  u64* ws;
};

__device__ __forceinline__ float bf2f(unsigned h) { return __builtin_bit_cast(float, h << 16); }
__device__ __forceinline__ unsigned cvtpk(float lo, float hi) {
  bf16x2_t r = {(__bf16)lo, (__bf16)hi};
  return __builtin_bit_cast(unsigned, r);
}
__device__ __forceinline__ float rbf(float f) { return bf2f(cvtpk(f, f) & 0xffffu); }
__device__ __forceinline__ float dot2(unsigned a, unsigned b, float acc) {
  return __builtin_amdgcn_fdot2_f32_bf16(__builtin_bit_cast(bf16x2_t, a), __builtin_bit_cast(bf16x2_t, b), acc, false);
}
template <int CTRL> __device__ __forceinline__ float dpp(float x) {
  return __builtin_bit_cast(float, __builtin_amdgcn_mov_dpp(__builtin_bit_cast(int, x), CTRL, 0xf, 0xf, true));
}
constexpr int XOR1 = 0xB1, XOR2 = 0x4E, HMIR = 0x141, ROR8 = 0x128;
__device__ __forceinline__ float sum8(float x) { x += dpp<XOR1>(x); x += dpp<XOR2>(x); x += dpp<HMIR>(x); return x; }
__device__ __forceinline__ float sum16(float x) { x = sum8(x); x += dpp<ROR8>(x); return x; }
__device__ __forceinline__ float max16(float x) {
  x = fmaxf(x, dpp<XOR1>(x)); x = fmaxf(x, dpp<XOR2>(x)); x = fmaxf(x, dpp<HMIR>(x)); x = fmaxf(x, dpp<ROR8>(x)); return x; }
__device__ __forceinline__ float xrow_sum(float x) {
  auto s = __builtin_amdgcn_permlane16_swap(__float_as_uint(x), __float_as_uint(x), false, false);
  x = __uint_as_float(s[0]) + __uint_as_float(s[1]);
  auto t = __builtin_amdgcn_permlane32_swap(__float_as_uint(x), __float_as_uint(x), false, false);
  return __uint_as_float(t[0]) + __uint_as_float(t[1]);
}
__device__ __forceinline__ float xrow_max(float x) {
  auto s = __builtin_amdgcn_permlane16_swap(__float_as_uint(x), __float_as_uint(x), false, false);
  x = fmaxf(__uint_as_float(s[0]), __uint_as_float(s[1]));
  auto t = __builtin_amdgcn_permlane32_swap(__float_as_uint(x), __float_as_uint(x), false, false);
  return fmaxf(__uint_as_float(t[0]), __uint_as_float(t[1]));
}
__device__ __forceinline__ f32x4 mfma(v4u a, v4u b, f32x4 c) {
  return __builtin_amdgcn_mfma_f32_16x16x32_bf16(__builtin_bit_cast(bf16x8, a), __builtin_bit_cast(bf16x8, b), c, 0, 0, 0);
}
__device__ __forceinline__ __amdgpu_buffer_rsrc_t mkrsrc(const void* p, unsigned bytes) {
  return __builtin_amdgcn_make_buffer_rsrc((void*)p, 0, bytes, RSRC_W3);
}
#define SB() __builtin_amdgcn_sched_barrier(0)
#define VMWAIT(n) asm volatile("s_waitcnt vmcnt(%0)" ::"n"(n) : "memory")
#define LGKM0() asm volatile("s_waitcnt lgkmcnt(0)" ::: "memory")
#define BAR() do { LGKM0(); __builtin_amdgcn_s_barrier(); asm volatile("" ::: "memory"); } while (0)

__device__ __forceinline__ void put_gran(gu64* g, float v, unsigned tlo, unsigned thi) {
  const unsigned hi = (__float_as_uint(v) & 0xffffff00u) | thi;
  __hip_atomic_store(g, ((u64)hi << 32) | tlo, RLX_AGENT);
}
__device__ __forceinline__ bool gran_ok(u64 g, unsigned tlo, unsigned thi) {
  return ((unsigned)g == tlo) & (((unsigned)(g >> 32) & 0xffu) == thi);
}
__device__ __forceinline__ float gran_val(u64 g) { return __uint_as_float((unsigned)(g >> 32) & 0xffffff00u); }

__device__ __forceinline__ void lds_wait_ge(LAS unsigned* f, unsigned n) {
  while (__hip_atomic_load(f, RLX_WG) < n) __builtin_amdgcn_s_sleep(1);
  asm volatile("" ::: "memory");
}
__device__ __forceinline__ void lds_signal(LAS unsigned* f, int lane) {
  LGKM0();
  if (lane == 0) (void)__hip_atomic_fetch_add(f, 1u, RLX_WG);
}

#define TS(i) do { if ((i) == 0 || (i) == PROBE) { SB(); tpr[(i) == 0 ? 0 : 1] = __builtin_amdgcn_s_memrealtime(); SB(); } } while (0)

__global__ void __launch_bounds__(NT) fused_decode(Params p) {
  __shared__ __attribute__((aligned(16))) float lds_[L_END];
  LAS float* lds = (LAS float*)lds_;
  LAS unsigned* flags = (LAS unsigned*)(lds + L_FLAG);
  const int tid = threadIdx.x, l = tid & 63, w = __builtin_amdgcn_readfirstlane(tid >> 6);
  const int b = blockIdx.x;
  const int s = b & 7, h = b >> 3;
  const int h2 = (b & 7) * 4 + (b >> 6), s2 = (b >> 3) & 7;
  u64 tpr[2] = {0, 0};
  TS(0);
  gu64* ws = (gu64*)p.ws;

  const unsigned voff = (unsigned)((l >> 3) * ROWB + (l & 7) * 16);
  const size_t kvoff = ((size_t)h * SK + (SK - WN) + (size_t)s * KEYS_WG) * DH;
  const __amdgpu_buffer_rsrc_t rk = mkrsrc(p.k + kvoff, KEYS_WG * ROWB);
  const __amdgpu_buffer_rsrc_t rv = mkrsrc(p.v + kvoff, KEYS_WG * ROWB);
  const int woff = w * 4 * 8 * ROWB;

  v4u mk;
  {
    const unsigned hm = (l & 1) ? 0xffff0000u : 0x0000ffffu;
    const int q4 = (l & 7) >> 1;
    mk.x = q4 == 0 ? hm : 0u; mk.y = q4 == 1 ? hm : 0u; mk.z = q4 == 2 ? hm : 0u; mk.w = q4 == 3 ? hm : 0u;
  }

  v4u K[12], V[12];
  u64 ep;

  if (w < 10) {
    const int row0 = 80 * s + 8 * w;
    const __amdgpu_buffer_rsrc_t rq = mkrsrc(p.wq + (size_t)row0 * (HQ * DH) + h * DH, 8 * HQ * DH * 2);
    const __amdgpu_buffer_rsrc_t r1 = mkrsrc(p.w1 + (size_t)row0 * FF + h * 48, 8 * FF * 2);
    const unsigned qoff = (unsigned)((l >> 3) * (HQ * DH * 2) + (l & 7) * 16);
    const unsigned w1off = (l & 7) < 6 ? (unsigned)((l >> 3) * (FF * 2) + (l & 7) * 16) : OOB;
    v4u A[4];
#pragma unroll
    for (int j = 0; j < 3; ++j) A[j] = __builtin_amdgcn_raw_buffer_load_b128(rq, qoff + j * 128, 0, W_AUX);
    A[3] = __builtin_amdgcn_raw_buffer_load_b128(r1, w1off, 0, W_AUX);
    const GAS unsigned* x32 = (const GAS unsigned*)p.x;
    unsigned xd[5];
#pragma unroll
    for (int i = 0; i < 5; ++i) xd[i] = x32[l + 64 * i];
    const unsigned xr = ((const GAS unsigned short*)p.x)[row0 + (l >> 3)];
    ep = __hip_atomic_load(ws + G_EPOCH, RLX_AGENT);
    SB();
#pragma unroll
    for (int i = 0; i < WIN; ++i) K[i] = __builtin_amdgcn_raw_buffer_load_b128(rk, voff + (i % 3) * 128, woff + (i / 3) * 8 * ROWB, KV_AUX);
    SB();
    if (tid == 0) { flags[0] = 0; flags[1] = 0; flags[2] = 0; flags[3] = 0; }
    TS(12);
    BAR();
    TS(13);
    float xs[10], sum = 0.f;
#pragma unroll
    for (int i = 0; i < 5; ++i) { xs[2 * i] = bf2f(xd[i] & 0xffffu); xs[2 * i + 1] = __uint_as_float(xd[i] & 0xffff0000u); sum += xs[2 * i] + xs[2 * i + 1]; }
    sum = xrow_sum(sum16(sum));
    const float mean = sum * (1.0f / DM);
    float sq = 0.f;
#pragma unroll
    for (int i = 0; i < 10; ++i) { const float d = xs[i] - mean; sq = fmaf(d, d, sq); }
    sq = xrow_sum(sum16(sq));
    const float sd = rbf(sqrtf(rbf(rbf(sq * (1.0f / DM)) + 1e-5f)));
    const float xn = rbf(bf2f(xr) - rbf(mean)) / sd;
    const unsigned xdup = cvtpk(xn, xn);
    const v4u sel = mk & xdup;
    const bool valid = ((l >> 3) & 1) == (l >> 5);
    LAS float* qp = lds + L_QP + (2 * w + (l >> 5)) * QP_STR + 8 * (l & 7) + 4 * ((l >> 4) & 1);
#pragma unroll
    for (int j = 0; j < 4; ++j) {
      const f32x4 d = mfma(sel, A[j], f32x4{0.f, 0.f, 0.f, 0.f});
      if (valid) *(LAS f32x4*)(qp + 64 * j) = d;
    }
    lds_signal(flags + 0, l);
    TS(1);
#pragma unroll
    for (int i = WIN; i < 12; ++i) {
      VMWAIT(WIN - 1);
      K[i] = __builtin_amdgcn_raw_buffer_load_b128(rk, voff + (i % 3) * 128, woff + (i / 3) * 8 * ROWB, KV_AUX);
      SB();
    }
  } else if (w < 12) {
#pragma unroll
    for (int i = 0; i < WIN; ++i) K[i] = __builtin_amdgcn_raw_buffer_load_b128(rk, voff + (i % 3) * 128, woff + (i / 3) * 8 * ROWB, KV_AUX);
    ep = __hip_atomic_load(ws + G_EPOCH, RLX_AGENT);
    SB();
    BAR();
#pragma unroll
    for (int i = WIN; i < 12; ++i) {
      VMWAIT(WIN - 1);
      K[i] = __builtin_amdgcn_raw_buffer_load_b128(rk, voff + (i % 3) * 128, woff + (i / 3) * 8 * ROWB, KV_AUX);
      SB();
    }
  } else {
    const int c = (w - 12) * 60 + l;
    const bool act = l < 60;
    K[0] = __builtin_amdgcn_raw_buffer_load_b128(rk, voff, woff, KV_AUX);
    ep = __hip_atomic_load(ws + G_EPOCH, RLX_AGENT);
#pragma unroll
    for (int i = 1; i < 12; ++i) K[i] = __builtin_amdgcn_raw_buffer_load_b128(rk, voff + (i % 3) * 128, woff + (i / 3) * 8 * ROWB, KV_AUX);
    SB();
    BAR();
    const u64 tag = ep + 1;
    const unsigned tlo = (unsigned)tag, thi = (unsigned)(tag >> 32) & 0xffu;
    lds_wait_ge(flags + 0, 10);
    TS(1);
    float acc = 0.f;
    const int cc = act ? c : 0;
#pragma unroll
    for (int i = 0; i < 20; ++i) acc += lds[L_QP + i * QP_STR + cc];
    if (act) put_gran(ws + G_Q + (size_t)(h * 8 + s) * 256 + c, acc, tlo, thi);
    TS(2);
    gu64* src = ws + G_Q + (size_t)((c < 192 ? h : h2) * 8) * 256 + cc;
    float tot;
    for (unsigned spins = 0;; ++spins) {
      u64 g[8];
#pragma unroll
      for (int i = 0; i < 8; ++i) g[i] = __hip_atomic_load(src + i * 256, RLX_AGENT);
      bool ok = true;
      tot = 0.f;
#pragma unroll
      for (int i = 0; i < 8; ++i) { ok &= gran_ok(g[i], tlo, thi); tot += gran_val(g[i]); }
      if (__all(ok | !act) || spins > SPIN_CAP) break;
      __builtin_amdgcn_s_sleep(2);
    }
    TS(3);
    if (act && c < 192) ((LAS unsigned short*)(lds + L_QB))[c] = (unsigned short)(cvtpk(tot, tot) & 0xffffu);
    lds_signal(flags + 1, l);
    if (w == 15) {
      v4u W2[12];
      const unsigned b1v = ((const GAS unsigned short*)p.b1)[h2 * 48 + (l >= 12 && act ? l - 12 : 0)];
      const __amdgpu_buffer_rsrc_t r2 = mkrsrc(p.w2 + (size_t)(h2 * 48) * DM + s2 * 80, 48 * DM * 2);
      const unsigned o2 = (l & 15) < 10 ? (unsigned)((l >> 4) * (DM * 2) + (l & 15) * 16) : OOB;
#pragma unroll
      for (int i = 0; i < 12; ++i) W2[i] = __builtin_amdgcn_raw_buffer_load_b128(r2, o2, i * 4 * DM * 2, W_AUX);
      SB();
      float hv = rbf(rbf(tot) + bf2f(b1v));
      hv = hv > 0.f ? hv : 0.f;
      hv = hv * hv;
      if (l >= 12 && act) ((LAS unsigned*)(lds + L_HB))[l - 12] = cvtpk(hv, hv);
      LGKM0();
      f32x4 z = {0.f, 0.f, 0.f, 0.f};
      const unsigned lm = (l & 8) ? 0u : 0xffffffffu;
#pragma unroll
      for (int i = 0; i < 12; ++i) {
        const unsigned hd = ((LAS unsigned*)(lds + L_HB))[4 * i + (l >> 4)] & lm;
        z = mfma(mk & hd, W2[i], z);
      }
      if ((l & 15) < 10 && l < 32) {
        gu64* dst = ws + G_Z + ((size_t)(s2 * 20 + 2 * (l & 15) + (l >> 4)) * 32 + h2) * 4;
#pragma unroll
        for (int r = 0; r < 4; ++r) put_gran(dst + r, z[r], tlo, thi);
      }
    }
  }
  const u64 tag = ep + 1;
  const unsigned tlo = (unsigned)tag, thi = (unsigned)(tag >> 32) & 0xffu;

  float sc[4];
  v4u qf[3];
#pragma unroll
  for (int g = 0; g < 4; ++g) {
#pragma unroll
    for (int j = 0; j < 3; ++j) {
      VMWAIT(WIN - 1);
      V[g * 3 + j] = __builtin_amdgcn_raw_buffer_load_b128(rv, voff + j * 128, woff + g * 8 * ROWB, KV_AUX);
      SB();
    }
    if (g == 0) {
      TS(4);
      lds_wait_ge(flags + 1, 4);
#pragma unroll
      for (int j = 0; j < 3; ++j) qf[j] = *(LAS v4u*)(lds + L_QB + 32 * j + 4 * (l & 7));
      TS(5);
    }
    float d0 = 0.f, d1 = 0.f;
#pragma unroll
    for (int j = 0; j < 3; ++j) {
      d0 = dot2(K[g * 3 + j].x, qf[j].x, d0); d1 = dot2(K[g * 3 + j].y, qf[j].y, d1);
      d0 = dot2(K[g * 3 + j].z, qf[j].z, d0); d1 = dot2(K[g * 3 + j].w, qf[j].w, d1);
    }
    sc[g] = rbf(rbf(sum8(d0 + d1)) * 0.07216878364870322f);
    SB();
  }
  v4u WO[3];
  {
    const __amdgpu_buffer_rsrc_t ro = mkrsrc(p.wo + (size_t)(h2 * DH + 12 * w) * DM + s2 * 80, 12 * DM * 2);
    const unsigned oo = (l & 15) < 10 ? (unsigned)((l >> 4) * (DM * 2) + (l & 15) * 16) : OOB;
#pragma unroll
    for (int i = 0; i < 3; ++i) { VMWAIT(WIN - 1); WO[i] = __builtin_amdgcn_raw_buffer_load_b128(ro, oo, i * 4 * DM * 2, W_AUX); SB(); }
  }
  unsigned xfin = 0, b2fin = 0;
  if (w == 5 && h2 < 20) {
    xfin = ((const GAS unsigned*)p.x)[s2 * 40 + h2 * 2 + (l & 1)];
    b2fin = ((const GAS unsigned*)p.b2)[s2 * 40 + h2 * 2 + (l & 1)];
  }
  float mw = fmaxf(fmaxf(sc[0], sc[1]), fmaxf(sc[2], sc[3]));
  mw = fmaxf(mw, dpp<ROR8>(mw));
  mw = xrow_max(mw);
  if (l == 0) lds[L_M + w] = mw;
  lds_signal(flags + 2, l);
  constexpr float LOG2E = 1.4426950408889634f;
  float pr[4], lw = 0.f;
#pragma unroll
  for (int g = 0; g < 4; ++g) { pr[g] = __builtin_amdgcn_exp2f((sc[g] - mw) * LOG2E); lw += pr[g]; }
  lw += dpp<ROR8>(lw);
  lw = xrow_sum(lw);
  TS(6);
  f32x4 acc[3] = {{0.f, 0.f, 0.f, 0.f}, {0.f, 0.f, 0.f, 0.f}, {0.f, 0.f, 0.f, 0.f}};
#pragma unroll
  for (int g = 0; g < 4; ++g) {
    const v4u sel = mk & cvtpk(pr[g], pr[g]);
#pragma unroll
    for (int j = 0; j < 3; ++j) acc[j] = mfma(sel, V[g * 3 + j], acc[j]);
  }
  lds_wait_ge(flags + 2, 16);
  const float mwg = max16(lds[L_M + (l & 15)]);
  const float fw = __builtin_amdgcn_exp2f((mw - mwg) * LOG2E);
  {
    const bool valid = ((l >> 3) & 1) == (l >> 5);
    LAS float* op = lds + L_OP + (w + 16 * (l >> 5)) * OP_STR + 8 * (l & 7) + 4 * ((l >> 4) & 1);
#pragma unroll
    for (int j = 0; j < 3; ++j) if (valid) *(LAS f32x4*)(op + 64 * j) = acc[j] * fw;
    if (l == 0) lds[L_L + w] = lw * fw;
  }
  TS(7);
  BAR();
  if (w < 12) {
    const int c4 = tid >> 4, pg = tid & 15;
    const f32x4 a = *(LAS f32x4*)(lds + L_OP + pg * OP_STR + 4 * c4);
    const f32x4 bq = *(LAS f32x4*)(lds + L_OP + (pg + 16) * OP_STR + 4 * c4);
    f32x4 t = a + bq;
#pragma unroll
    for (int r = 0; r < 4; ++r) t[r] = sum16(t[r]);
    const float vsel = pg == 0 ? t[0] : pg == 1 ? t[1] : pg == 2 ? t[2] : t[3];
    if (pg < 4) put_gran(ws + G_O + (size_t)(h * 8 + s) * 256 + 4 * c4 + pg, vsel, tlo, thi);
  } else if (w == 12) {
    const float lsum = sum16(lds[L_L + (l & 15)]);
    if (l < 2) put_gran(ws + G_O + (size_t)(h * 8 + s) * 256 + 192 + l, l == 0 ? mwg : lsum, tlo, thi);
  }
  TS(8);
  if (w >= 13) {
    const int d = (w - 13) * 64 + l;
    gu64* src = ws + G_O + (size_t)(h2 * 8) * 256;
    u64 g[8], gm;
    for (unsigned spins = 0;; ++spins) {
#pragma unroll
      for (int i = 0; i < 8; ++i) g[i] = __hip_atomic_load(src + i * 256 + d, RLX_AGENT);
      gm = __hip_atomic_load(src + ((l >> 1) & 7) * 256 + 192 + (l & 1), RLX_AGENT);
      bool ok = gran_ok(gm, tlo, thi);
#pragma unroll
      for (int i = 0; i < 8; ++i) ok &= gran_ok(g[i], tlo, thi);
      if (__all(ok) || spins > SPIN_CAP) break;
      __builtin_amdgcn_s_sleep(1);
    }
    const float ml = gran_val(gm);
    const float mo = dpp<XOR1>(ml);
    const float ms = (l & 1) ? mo : ml, ls = (l & 1) ? ml : mo;
    const float M = max16(ms);
    const float e = __builtin_amdgcn_exp2f((ms - M) * LOG2E);
    const float L = 0.5f * sum16(e * ls);
    const float f = e / L;
    float o = 0.f;
#pragma unroll
    for (int i = 0; i < 8; ++i) o = fmaf(__builtin_bit_cast(float, __builtin_amdgcn_readlane(__builtin_bit_cast(int, f), 2 * i)), gran_val(g[i]), o);
    ((LAS unsigned short*)(lds + L_OB))[d] = (unsigned short)(cvtpk(o, o) & 0xffffu);
  }
  TS(9);
  BAR();
  {
    f32x4 y = {0.f, 0.f, 0.f, 0.f};
    const unsigned lm = (l & 8) ? 0u : 0xffffffffu;
#pragma unroll
    for (int i = 0; i < 3; ++i) {
      const unsigned ov = ((LAS unsigned short*)(lds + L_OB))[12 * w + 4 * i + (l >> 4)];
      y = mfma(mk & ((ov | (ov << 16)) & lm), WO[i], y);
    }
    if ((l & 15) < 10 && l < 32) *(LAS f32x4*)(lds + L_YP + w * YP_STR + 8 * (l & 15) + 4 * (l >> 4)) = y;
  }
  BAR();
  if (w < 5) {
    const int c4 = tid >> 4, pg = tid & 15;
    f32x4 t = *(LAS f32x4*)(lds + L_YP + pg * YP_STR + 4 * c4);
#pragma unroll
    for (int r = 0; r < 4; ++r) t[r] = sum16(t[r]);
    const float vsel = pg == 0 ? t[0] : pg == 1 ? t[1] : pg == 2 ? t[2] : t[3];
    if (pg < 4) put_gran(ws + G_Y + ((size_t)(s2 * 20 + c4) * 32 + h2) * 4 + pg, vsel, tlo, thi);
    TS(10);
  } else if (w == 5 && h2 < 20) {
    gu64* ys = ws + G_Y + (size_t)(s2 * 20 + h2) * 128 + 2 * l;
    gu64* zs = ws + G_Z + (size_t)(s2 * 20 + h2) * 128 + 2 * l;
    u64 y0, y1, z0, z1;
    for (unsigned spins = 0;; ++spins) {
      y0 = __hip_atomic_load(ys, RLX_AGENT); y1 = __hip_atomic_load(ys + 1, RLX_AGENT);
      z0 = __hip_atomic_load(zs, RLX_AGENT); z1 = __hip_atomic_load(zs + 1, RLX_AGENT);
      const bool ok = gran_ok(y0, tlo, thi) & gran_ok(y1, tlo, thi) & gran_ok(z0, tlo, thi) & gran_ok(z1, tlo, thi);
      if (__all(ok) || spins > SPIN_CAP) break;
      __builtin_amdgcn_s_sleep(1);
    }
    float v4[4] = {gran_val(y0), gran_val(y1), gran_val(z0), gran_val(z1)};
#pragma unroll
    for (int r = 0; r < 4; ++r) {
      float t = v4[r];
      t += dpp<XOR2>(t);
      const float u = dpp<HMIR>(t);
      t += dpp<XOR1>(u);
      t += dpp<ROR8>(t);
      v4[r] = xrow_sum(t);
    }
    float res[2];
#pragma unroll
    for (int r = 0; r < 2; ++r) {
      const float sv = rbf(v4[r]);
      const float bb = r == 0 ? bf2f(b2fin & 0xffffu) : __uint_as_float(b2fin & 0xffff0000u);
      const float xx = r == 0 ? bf2f(xfin & 0xffffu) : __uint_as_float(xfin & 0xffff0000u);
      const float o2 = rbf(rbf(v4[2 + r]) + bb);
      const float gt = rbf(o2 / (1.0f + __expf(-o2)));
      res[r] = xx + rbf(sv * gt);
    }
    if (b == 0 && l == 0) __hip_atomic_store(ws + 8, tpr[0], RLX_AGENT);
    if (l < 2) ((GAS unsigned*)p.out)[s2 * 40 + h2 * 2 + l] = cvtpk(res[0], res[1]);
    if (b == 0 && l == 0) __hip_atomic_store(ws + G_EPOCH, tag, RLX_AGENT);
    TS(11);
  }
}

__global__ void k_post(u64* ws) { const u64 tend = __hip_atomic_load((gu64*)ws + 8, RLX_AGENT) + 4000; while (__builtin_amdgcn_s_memrealtime() < tend) __builtin_amdgcn_s_sleep(1); }
extern "C" void kernel_launch(void* const* d_in, const int* in_sizes, int n_in,
                              void* d_out, int out_size, void* d_ws, size_t ws_size,
                              hipStream_t stream) {
  Params p;
  p.x = (const unsigned short*)d_in[0];
  p.k = (const unsigned short*)d_in[1];
  p.v = (const unsigned short*)d_in[2];
  p.wq = (const unsigned short*)d_in[3];
  p.wo = (const unsigned short*)d_in[4];
  p.w1 = (const unsigned short*)d_in[6];
  p.b1 = (const unsigned short*)d_in[7];
  p.w2 = (const unsigned short*)d_in[8];
  p.b2 = (const unsigned short*)d_in[9];
  p.out = (unsigned short*)d_out;
  p.ws = (u64*)d_ws;
  hipLaunchKernelGGL(fused_decode, dim3(NWG), dim3(NT), 0, stream, p);
  hipLaunchKernelGGL(k_post, dim3(1), dim3(64), 0, stream, (u64*)d_ws);
}
```

```cpp
#include <hip/hip_runtime.h>
#include <hip/hip_bf16.h>
#include <stdint.h>

typedef unsigned v4u __attribute__((ext_vector_type(4)));
typedef float f32x4 __attribute__((ext_vector_type(4)));
typedef short bf16x8 __attribute__((ext_vector_type(8)));
typedef __bf16 bf16x2_t __attribute__((ext_vector_type(2)));
typedef unsigned long long u64;
#define GAS __attribute__((address_space(1)))
#define LAS __attribute__((address_space(3)))
#define RLX_AGENT __ATOMIC_RELAXED, __HIP_MEMORY_SCOPE_AGENT
#define RLX_WG __ATOMIC_RELAXED, __HIP_MEMORY_SCOPE_WORKGROUP
#define CAS __attribute__((address_space(4)))
typedef GAS u64 gu64;

#define WIN 2
#define WIN0 2
#define QI 4
#define GI 1
#define EI 12
#define WINE 4
#define LATEW 16
#define WOPOS 12
#define WOGATE 16
#define PUBPOS 5
#define YZPACK 1
constexpr int KV_AUX = 2, W_AUX = 2;

constexpr int DM = 640, HQ = 32, DH = 192, SK = 32768, WN = 4096, FF = 1536;
constexpr int NWG = 256, NT = 1024;
constexpr int ROWB = DH * 2;
constexpr int GRPB = 8 * ROWB;
constexpr float LOG2E = 1.4426950408889634f;
constexpr unsigned OOB = 0x40000000u;
constexpr int RSRC_W3 = 0x00020000;
constexpr unsigned SPIN_CAP = 1u << 18;

constexpr size_t G_EPOCH = 0;
constexpr size_t G_Q = 64;
constexpr size_t G_O = G_Q + 256 * 64;
constexpr size_t G_Y = G_O + (size_t)NWG * 128;
constexpr size_t G_END = G_Y + (size_t)NWG * 160;
constexpr size_t G_TS = G_END + 64;

constexpr int QP_STR = 68;
constexpr int OP_STR = 200;
constexpr int YP_STR = 164;
constexpr int L_QP = 0;
constexpr int L_OP = L_QP + 20 * QP_STR;
constexpr int L_YP = L_OP + 32 * OP_STR;
constexpr int L_X = L_YP + 16 * YP_STR;
constexpr int L_QB = L_X + 512;
constexpr int L_OB = L_QB + 96;
constexpr int L_HB = L_OB + 96;
constexpr int L_M = L_HB + 48;
constexpr int L_L = L_M + 16;
constexpr int L_FLAG = L_L + 16;
constexpr int L_END = L_FLAG + 4;

struct Params {
  const unsigned short* x;
  const unsigned short* k;
  const unsigned short* v;
  const unsigned short* wq;
  const unsigned short* wo;
  const unsigned short* w1;
  const unsigned short* b1;
  const unsigned short* w2;
  const unsigned short* b2;
  unsigned short* out;
  u64* ws;
};

__device__ __forceinline__ float bf2f(unsigned h) { return __builtin_bit_cast(float, h << 16); }
__device__ __forceinline__ unsigned cvtpk(float lo, float hi) {
  bf16x2_t r = {(__bf16)lo, (__bf16)hi};
  return __builtin_bit_cast(unsigned, r);
}
__device__ __forceinline__ float rbf(float f) { return bf2f(cvtpk(f, f) & 0xffffu); }
__device__ __forceinline__ float dot2(unsigned a, unsigned b, float acc) {
  return __builtin_amdgcn_fdot2_f32_bf16(__builtin_bit_cast(bf16x2_t, a), __builtin_bit_cast(bf16x2_t, b), acc, false);
}
template <int CTRL> __device__ __forceinline__ float dpp(float x) {
  return __builtin_bit_cast(float, __builtin_amdgcn_mov_dpp(__builtin_bit_cast(int, x), CTRL, 0xf, 0xf, true));
}
constexpr int XOR1 = 0xB1, XOR2 = 0x4E, HMIR = 0x141, ROR8 = 0x128;
__device__ __forceinline__ float sum8(float x) { x += dpp<XOR1>(x); x += dpp<XOR2>(x); x += dpp<HMIR>(x); return x; }
__device__ __forceinline__ float sum16(float x) { x = sum8(x); x += dpp<ROR8>(x); return x; }
__device__ __forceinline__ float max16(float x) {
  x = fmaxf(x, dpp<XOR1>(x)); x = fmaxf(x, dpp<XOR2>(x)); x = fmaxf(x, dpp<HMIR>(x)); x = fmaxf(x, dpp<ROR8>(x)); return x; }
__device__ __forceinline__ float xrow_sum(float x) {
  auto s = __builtin_amdgcn_permlane16_swap(__float_as_uint(x), __float_as_uint(x), false, false);
  x = __uint_as_float(s[0]) + __uint_as_float(s[1]);
  auto t = __builtin_amdgcn_permlane32_swap(__float_as_uint(x), __float_as_uint(x), false, false);
  return __uint_as_float(t[0]) + __uint_as_float(t[1]);
}
__device__ __forceinline__ float xrow_max(float x) {
  auto s = __builtin_amdgcn_permlane16_swap(__float_as_uint(x), __float_as_uint(x), false, false);
  x = fmaxf(__uint_as_float(s[0]), __uint_as_float(s[1]));
  auto t = __builtin_amdgcn_permlane32_swap(__float_as_uint(x), __float_as_uint(x), false, false);
  return fmaxf(__uint_as_float(t[0]), __uint_as_float(t[1]));
}
__device__ __forceinline__ f32x4 mfma(v4u a, v4u b, f32x4 c) {
  return __builtin_amdgcn_mfma_f32_16x16x32_bf16(__builtin_bit_cast(bf16x8, a), __builtin_bit_cast(bf16x8, b), c, 0, 0, 0);
}
__device__ __forceinline__ __amdgpu_buffer_rsrc_t mkrsrc(const void* p, unsigned bytes) {
  return __builtin_amdgcn_make_buffer_rsrc((void*)p, 0, bytes, RSRC_W3);
}
#define SB() __builtin_amdgcn_sched_barrier(0)
#define VMWAIT(n) asm volatile("s_waitcnt vmcnt(%0)" ::"n"(n) : "memory")
#define LGKM0() asm volatile("s_waitcnt lgkmcnt(0)" ::: "memory")
#define BAR() do { LGKM0(); __builtin_amdgcn_s_barrier(); asm volatile("" ::: "memory"); } while (0)

__device__ __forceinline__ void put_gran(gu64* g, float v, unsigned tlo, unsigned thi) {
  const unsigned hi = (__float_as_uint(v) & 0xffffff00u) | thi;
  __hip_atomic_store(g, ((u64)hi << 32) | tlo, RLX_AGENT);
}
__device__ __forceinline__ bool gran_ok(u64 g, unsigned tlo, unsigned thi) {
  return ((unsigned)g == tlo) & (((unsigned)(g >> 32) & 0xffu) == thi);
}
__device__ __forceinline__ float gran_val(u64 g) { return __uint_as_float((unsigned)(g >> 32) & 0xffffff00u); }
__device__ __forceinline__ void put_gran2(gu64* g, unsigned v, unsigned tlo) { __hip_atomic_store(g, ((u64)v << 32) | tlo, RLX_AGENT); }
__device__ __forceinline__ u64 get_gran(gu64* g) { return __hip_atomic_load(g, RLX_AGENT); }

__device__ __forceinline__ void lds_wait_ge(LAS unsigned* f, unsigned n) {
  while (__hip_atomic_load(f, RLX_WG) < n) __builtin_amdgcn_s_sleep(1);
  asm volatile("" ::: "memory");
}
__device__ __forceinline__ int opaque0() { int z; asm("v_mov_b32 %0, 0" : "=v"(z)); return z; }
__device__ __forceinline__ void lds_signal(LAS unsigned* f, int lane) {
  LGKM0();
  if (lane == 0) (void)__hip_atomic_fetch_add(f + opaque0(), 1u, RLX_WG);
}
__device__ __forceinline__ float qk_score(v4u k0, v4u k1, v4u k2, const v4u (&qf)[3]) {
  float d0 = dot2(k0.x, qf[0].x, 0.f), d1 = dot2(k0.y, qf[0].y, 0.f);
  d0 = dot2(k0.z, qf[0].z, d0); d1 = dot2(k0.w, qf[0].w, d1);
  d0 = dot2(k1.x, qf[1].x, d0); d1 = dot2(k1.y, qf[1].y, d1);
  d0 = dot2(k1.z, qf[1].z, d0); d1 = dot2(k1.w, qf[1].w, d1);
  d0 = dot2(k2.x, qf[2].x, d0); d1 = dot2(k2.y, qf[2].y, d1);
  d0 = dot2(k2.z, qf[2].z, d0); d1 = dot2(k2.w, qf[2].w, d1);
  return rbf(rbf(sum8(d0 + d1)) * 0.07216878364870322f);
}
#define KLOAD(i) __builtin_amdgcn_raw_buffer_load_b128(rk, voff + ((i) % 3) * 128, woff + ((i) / 3) * GRPB, KV_AUX)
#define VLOAD(i) __builtin_amdgcn_raw_buffer_load_b128(rv, voff + ((i) % 3) * 128, woff + ((i) / 3) * GRPB, KV_AUX)

#define TS(i) do {} while (0)

__global__ void __launch_bounds__(NT) fused_decode(Params p) {
  __shared__ __attribute__((aligned(16))) float lds_[L_END];
  LAS float* lds = (LAS float*)lds_;
  LAS unsigned* flags = (LAS unsigned*)(lds + L_FLAG);
  const int tid = threadIdx.x, l = tid & 63, w = __builtin_amdgcn_readfirstlane(tid >> 6);
  const int b = blockIdx.x;
  const int s = (b >> 3) & 7, h = (b & 7) * 4 + (b >> 6);
  TS(0);
  gu64* ws = (gu64*)p.ws;

  const unsigned voff = (unsigned)((l >> 3) * ROWB + (l & 7) * 16);
  const size_t kvoff = ((size_t)h * SK + (SK - WN) + (size_t)s * (WN / 8)) * DH;
  const __amdgpu_buffer_rsrc_t rk = mkrsrc(p.k + kvoff, (WN / 8) * ROWB);
  const __amdgpu_buffer_rsrc_t rv = mkrsrc(p.v + kvoff, (WN / 8) * ROWB);
  const int woff = w * (4 * GRPB);

  const bool proj = w < 10 && b < 240;
  const int row0 = (b & 1) * 320 + 32 * w;
  v4u K[12], V[12], A[4], X0, X1;
  if (__builtin_expect(proj, 1)) {
    const int ld = b < 192 ? HQ * DH : FF;
    const unsigned short* wb = b < 192 ? p.wq + (b >> 1) * 64 : p.w1 + ((b - 192) >> 1) * 64;
    const __amdgpu_buffer_rsrc_t ra = mkrsrc(wb + (size_t)row0 * ld, 32 * ld * 2);
    const __amdgpu_buffer_rsrc_t rx = mkrsrc(p.x, DM * 2);
    const unsigned aoff = (unsigned)((l >> 3) * ld * 2 + (l & 7) * 16);
    X0 = __builtin_amdgcn_raw_buffer_load_b128(rx, l * 16, 0, 0);
    X1 = __builtin_amdgcn_raw_buffer_load_b128(rx, l * 16 + 1024, 0, 0);
#pragma unroll
    for (int i = 0; i < 4; ++i) A[i] = __builtin_amdgcn_raw_buffer_load_b128(ra, aoff, i * 8 * ld * 2, W_AUX);
  }
  SB();
#pragma unroll
  for (int i = 0; i < WIN0; ++i) K[i] = KLOAD(i);
  SB();
  if (tid == 0) { flags[0] = 0; flags[1] = 0; flags[2] = 0; flags[3] = 0; }
  TS(12);
  BAR();
  TS(13);
  const u64 epv = get_gran(ws + G_EPOCH);
  SB();

  v4u mk;
  {
    const unsigned hm = (l & 1) ? 0xffff0000u : 0x0000ffffu;
    const int q4 = (l & 7) >> 1;
    mk.x = q4 == 0 ? hm : 0u; mk.y = q4 == 1 ? hm : 0u; mk.z = q4 == 2 ? hm : 0u; mk.w = q4 == 3 ? hm : 0u;
  }
  const bool valid = ((l >> 3) & 1) == (l >> 5);

  if (proj) {
    *(LAS v4u*)(lds + L_X + 4 * l) = X0;
    *(LAS v4u*)(lds + L_X + 256 + 4 * l) = X1;
    unsigned xr[4];
#pragma unroll
    for (int i = 0; i < 4; ++i) xr[i] = ((LAS unsigned short*)(lds + L_X))[row0 + 8 * i + (l >> 3)];
    float xs[16], sum = 0.f;
#pragma unroll
    for (int i = 0; i < 4; ++i) {
      xs[2 * i] = bf2f(X0[i] & 0xffffu); xs[2 * i + 1] = __uint_as_float(X0[i] & 0xffff0000u);
      xs[8 + 2 * i] = bf2f(X1[i] & 0xffffu); xs[8 + 2 * i + 1] = __uint_as_float(X1[i] & 0xffff0000u);
    }
#pragma unroll
    for (int i = 0; i < 8; ++i) sum += xs[i] + xs[8 + i];
    sum = xrow_sum(sum16(sum));
    const float mean = sum * (1.0f / DM);
    float sq = 0.f, sq1 = 0.f;
#pragma unroll
    for (int i = 0; i < 8; ++i) { const float d = xs[i] - mean, e = xs[8 + i] - mean; sq = fmaf(d, d, sq); sq1 = fmaf(e, e, sq1); }
    sq += l < 16 ? sq1 : 0.f;
    sq = xrow_sum(sum16(sq));
    const float sd = rbf(sqrtf(rbf(rbf(sq * (1.0f / DM)) + 1e-5f)));
    const float mb = rbf(mean);
    f32x4 d = {0.f, 0.f, 0.f, 0.f};
#pragma unroll
    for (int i = 0; i < 4; ++i) {
      const float xn = rbf(bf2f(xr[i]) - mb) / sd;
      d = mfma(mk & cvtpk(xn, xn), A[i], d);
    }
    if (valid) *(LAS f32x4*)(lds + L_QP + (2 * w + (l >> 5)) * QP_STR + 8 * (l & 7) + 4 * ((l >> 4) & 1)) = d;
    lds_signal(flags + 0, l);
    TS(1);
  }

  u64 tag;
#pragma unroll
  for (int i = WIN0; i < 12; ++i) {
    VMWAIT(WIN - 1);
    K[i] = KLOAD(i);
    SB();
    if (i == PUBPOS) {
      tag = ((u64)__builtin_amdgcn_readfirstlane((unsigned)(epv >> 32)) << 32 | (u64)__builtin_amdgcn_readfirstlane((unsigned)epv)) + 1;
      if (w == 12 && b < 240) {
        lds_wait_ge(flags + 0, 10);
        float acc = 0.f;
#pragma unroll
        for (int u = 0; u < 20; ++u) acc += lds[L_QP + u * QP_STR + l];
        put_gran(ws + G_Q + b * 64 + l, acc, (unsigned)tag, (unsigned)(tag >> 32) & 0xffu);
        TS(2);
      }
      SB();
    }
  }
  const unsigned tlo = (unsigned)tag, thi = (unsigned)(tag >> 32) & 0xffu;

  u64 g0 = 0, g1 = 0;
  unsigned b1v = 0;
  const int qcol = w < 15 ? h * DH + (w - 12) * 64 + l : HQ * DH + h * 48 + (l < 48 ? l : 0);
  gu64* qsrc = ws + G_Q + (qcol >> 6) * 128 + (qcol & 63);
  if (w >= 12) {
    g0 = get_gran(qsrc); g1 = get_gran(qsrc + 64);
    b1v = ((const GAS unsigned*)p.b1)[h * 24 + (l < 48 ? l >> 1 : 0)];
  }
  SB();

  float sc[4], mwg;
  v4u qf[3], sel[4], WO[3], W2;
  f32x4 acc[3] = {{0.f, 0.f, 0.f, 0.f}, {0.f, 0.f, 0.f, 0.f}, {0.f, 0.f, 0.f, 0.f}};
  unsigned xfin = 0, b2fin = 0;
  auto load_wo = [&](bool paced) {
    const __amdgpu_buffer_rsrc_t ro = mkrsrc(p.wo + (size_t)(h * DH + 12 * w) * DM + s * 80, 12 * DM * 2);
    const __amdgpu_buffer_rsrc_t r2 = mkrsrc(p.w2 + (size_t)(h * 48 + 4 * (w < 12 ? w : 0)) * DM + s * 80, w < 12 ? 4 * DM * 2 : 0);
    const unsigned oo = (l & 15) < 10 ? (unsigned)((l >> 4) * (DM * 2) + (l & 15) * 16) : OOB;
    if (paced) VMWAIT(WIN - 1);
    W2 = __builtin_amdgcn_raw_buffer_load_b128(r2, oo, 0, W_AUX);
    WO[0] = __builtin_amdgcn_raw_buffer_load_b128(ro, oo, 0, W_AUX);
    SB();
    if (paced) VMWAIT(WIN);
    WO[1] = __builtin_amdgcn_raw_buffer_load_b128(ro, oo, 4 * DM * 2, W_AUX);
    SB();
    if (paced) VMWAIT(WIN);
    WO[2] = __builtin_amdgcn_raw_buffer_load_b128(ro, oo, 8 * DM * 2, W_AUX);
    if (w == 10 && h < 20) {
      xfin = ((const GAS unsigned*)p.x)[s * 40 + h * 2 + (l & 1)];
      b2fin = ((const GAS unsigned*)p.b2)[s * 40 + h * 2 + (l & 1)];
    }
    SB();
  };
#pragma unroll
  for (int i = 0; i < 12; ++i) {
    if (i >= EI) VMWAIT(WINE - 1);
    else if (LATEW < 16 && w >= LATEW) VMWAIT(WIN);
    else VMWAIT(WIN - 1);
    V[i] = VLOAD(i);
    SB();
    if (i == GI && w >= 12) {
      if (__builtin_expect(!__all(gran_ok(g0, tlo, thi) & gran_ok(g1, tlo, thi)), 0)) {
        for (unsigned spins = 0; spins < SPIN_CAP; ++spins) {
          __builtin_amdgcn_s_sleep(4);
          g0 = get_gran(qsrc); g1 = get_gran(qsrc + 64);
          if (__all(gran_ok(g0, tlo, thi) & gran_ok(g1, tlo, thi))) break;
        }
      }
      const float tot = gran_val(g0) + gran_val(g1);
      if (w < 15) {
        ((LAS unsigned short*)(lds + L_QB))[(w - 12) * 64 + l] = (unsigned short)(cvtpk(tot, tot) & 0xffffu);
      } else {
        float hv = rbf(rbf(tot) + ((l & 1) ? __uint_as_float(b1v & 0xffff0000u) : bf2f(b1v & 0xffffu)));
        hv = hv > 0.f ? hv : 0.f;
        hv = hv * hv;
        if (l < 48) ((LAS unsigned*)(lds + L_HB))[l] = cvtpk(hv, hv);
      }
      lds_signal(flags + 1, l);
      TS(3);
    }
    if (i == QI) {
      TS(4);
      lds_wait_ge(flags + 1, 4);
#pragma unroll
      for (int j = 0; j < 3; ++j) qf[j] = *(LAS v4u*)(lds + L_QB + 32 * j + 4 * (l & 7));
      TS(5);
      sc[0] = qk_score(K[0], K[1], K[2], qf);
      sc[1] = qk_score(K[3], K[4], K[5], qf);
    }
    if (i == QI + 1) {
      sc[2] = qk_score(K[6], K[7], K[8], qf);
      sc[3] = qk_score(K[9], K[10], K[11], qf);
      float mw = fmaxf(fmaxf(sc[0], sc[1]), fmaxf(sc[2], sc[3]));
      mw = xrow_max(fmaxf(mw, dpp<ROR8>(mw)));
      if (l == 0) lds[L_M + w] = mw;
      lds_signal(flags + 2, l);
    }
    if (i == QI + 3) {
      lds_wait_ge(flags + 2, 16);
      mwg = max16(lds[L_M + (l & 15)]);
      float lw = 0.f;
#pragma unroll
      for (int g = 0; g < 4; ++g) {
        const float pr = __builtin_amdgcn_exp2f((sc[g] - mwg) * LOG2E);
        lw += pr;
        sel[g] = mk & cvtpk(pr, pr);
      }
      lw = xrow_sum(lw + dpp<ROR8>(lw));
      if (l == 0) lds[L_L + w] = lw;
#pragma unroll
      for (int u = 0; u < 6; ++u) acc[u % 3] = mfma(sel[u / 3], V[u], acc[u % 3]);
      TS(6);
    }
    if (i == 10) {
#pragma unroll
      for (int u = 6; u < 9; ++u) acc[u % 3] = mfma(sel[2], V[u], acc[u % 3]);
    }
    SB();
    if (i == WOPOS) load_wo(true);
  }
  if (WOPOS >= 12) lds_signal(flags + 3, l);
#pragma unroll
  for (int u = 9; u < 12; ++u) acc[u % 3] = mfma(sel[3], V[u], acc[u % 3]);
  {
    LAS float* op = lds + L_OP + (w + 16 * (l >> 5)) * OP_STR + 8 * (l & 7) + 4 * ((l >> 4) & 1);
#pragma unroll
    for (int j = 0; j < 3; ++j) if (valid) *(LAS f32x4*)(op + 64 * j) = acc[j];
  }
  TS(7);
  if (WOPOS >= 12) {
    lds_wait_ge(flags + 3, WOGATE);
    load_wo(false);
  }
  TS(15);
  BAR();
  gu64* og = ws + G_O + (size_t)(h * 8) * 128;
  if (w < 6) {
    const int c4 = tid >> 3, pg = tid & 7;
    LAS float* src = lds + L_OP + pg * OP_STR + 4 * c4;
    f32x4 t = (*(LAS f32x4*)(src) + *(LAS f32x4*)(src + 8 * OP_STR)) + (*(LAS f32x4*)(src + 16 * OP_STR) + *(LAS f32x4*)(src + 24 * OP_STR));
#pragma unroll
    for (int r = 0; r < 4; ++r) t[r] = sum8(t[r]);
    const unsigned pk = (pg & 1) ? cvtpk(t[2], t[3]) : cvtpk(t[0], t[1]);
    if (pg < 2) put_gran2(og + s * 128 + 2 * c4 + pg, pk, tlo);
  } else if (w == 6) {
    const float lsum = sum16(lds[L_L + (l & 15)]);
    if (l < 16) put_gran(og + s * 128 + 96 + l, l == 0 ? mwg : lsum, tlo, thi);
  }
  TS(8);
  if (w >= 14) {
    const int d = (w - 14) * 48 + (l < 48 ? l : 0);
    u64 g[8], gm;
    unsigned miss = 0x1ffu;
    for (unsigned spins = 0;; ++spins) {
#pragma unroll
      for (int i = 0; i < 8; ++i) if (miss >> i & 1u) g[i] = get_gran(og + i * 128 + d);
      if (miss >> 8) gm = get_gran(og + ((l >> 1) & 7) * 128 + 96 + (l & 1));
      unsigned m2 = __all(gran_ok(gm, tlo, thi)) ? 0u : 0x100u;
#pragma unroll
      for (int i = 0; i < 8; ++i) m2 |= __all((unsigned)g[i] == tlo) ? 0u : 1u << i;
      miss = m2;
      if (miss == 0 || spins > SPIN_CAP) break;
    }
    const float ml = gran_val(gm);
    const float mo = dpp<XOR1>(ml);
    const float ms = (l & 1) ? mo : ml, ls = (l & 1) ? ml : mo;
    const float M = max16(ms);
    const float e = __builtin_amdgcn_exp2f((ms - M) * LOG2E);
    const float L = 0.5f * sum16(e * ls);
    const float f = e / L;
    float o0 = 0.f, o1 = 0.f;
#pragma unroll
    for (int i = 0; i < 8; ++i) {
      const float fi = __builtin_bit_cast(float, __builtin_amdgcn_readlane(__builtin_bit_cast(int, f), 2 * i));
      const unsigned v = (unsigned)(g[i] >> 32);
      o0 = fmaf(fi, bf2f(v & 0xffffu), o0);
      o1 = fmaf(fi, __uint_as_float(v & 0xffff0000u), o1);
    }
    if (l < 48) ((LAS unsigned*)(lds + L_OB))[d] = cvtpk(o0, o1);
  }
  f32x4 y;
  {
    const unsigned hd = ((LAS unsigned*)(lds + L_HB))[4 * (w < 12 ? w : 0) + (l >> 4)];
    y = mfma(mk & ((l & 8) ? hd : 0u), W2, f32x4{0.f, 0.f, 0.f, 0.f});
    asm volatile("" : "+v"(y));
  }
  TS(9);
  BAR();
  {
#pragma unroll
    for (int i = 0; i < 3; ++i) {
      const unsigned ov = ((LAS unsigned short*)(lds + L_OB))[12 * w + 4 * i + (l >> 4)];
      y = mfma(mk & ((l & 8) ? 0u : (ov | (ov << 16))), WO[i], y);
    }
    if ((l & 15) < 10) *(LAS f32x4*)(lds + L_YP + w * YP_STR + 80 * (l >> 5) + 8 * (l & 15) + 4 * ((l >> 4) & 1)) = y;
  }
  BAR();
  if (w < 5) {
    const int c4 = tid >> 4, pg = tid & 15;
    f32x4 t = *(LAS f32x4*)(lds + L_YP + pg * YP_STR + 4 * c4), u = *(LAS f32x4*)(lds + L_YP + pg * YP_STR + 80 + 4 * c4);
#pragma unroll
    for (int r = 0; r < 4; ++r) { t[r] = sum16(t[r]); u[r] = sum16(u[r]); }
    const float vy = pg == 0 ? t[0] : pg == 1 ? t[1] : pg == 2 ? t[2] : t[3];
    const float vz = pg == 0 ? u[0] : pg == 1 ? u[1] : pg == 2 ? u[2] : u[3];
    if (pg < 4) put_gran2(ws + G_Y + (size_t)(s * 32 + h) * 160 + 4 * c4 + pg, cvtpk(vy, vz), tlo);
    TS(10);
  } else if (w == 10 && h < 20) {
    gu64* ys = ws + G_Y + (size_t)(s * 32 + (l >> 1)) * 160 + 4 * h + 2 * (l & 1);
    u64 y0, y1;
    for (unsigned spins = 0;; ++spins) {
      y0 = get_gran(ys); y1 = get_gran(ys + 1);
      if (__all(((unsigned)y0 == tlo) & ((unsigned)y1 == tlo)) || spins > SPIN_CAP) break;
    }
    const unsigned p0 = (unsigned)(y0 >> 32), p1 = (unsigned)(y1 >> 32);
    float v4[4] = {bf2f(p0 & 0xffffu), bf2f(p1 & 0xffffu), __uint_as_float(p0 & 0xffff0000u), __uint_as_float(p1 & 0xffff0000u)};
#pragma unroll
    for (int r = 0; r < 4; ++r) {
      float t = v4[r];
      t += dpp<XOR2>(t);
      const float u = dpp<HMIR>(t);
      t += dpp<XOR1>(u);
      t += dpp<ROR8>(t);
      v4[r] = xrow_sum(t);
    }
    float res[2];
#pragma unroll
    for (int r = 0; r < 2; ++r) {
      const float sv = rbf(v4[r]);
      const float bb = r == 0 ? bf2f(b2fin & 0xffffu) : __uint_as_float(b2fin & 0xffff0000u);
      const float xx = r == 0 ? bf2f(xfin & 0xffffu) : __uint_as_float(xfin & 0xffff0000u);
      const float o2 = rbf(rbf(v4[2 + r]) + bb);
      const float gt = rbf(o2 / (1.0f + __expf(-o2)));
      res[r] = xx + rbf(sv * gt);
    }
    if (l < 2) ((GAS unsigned*)p.out)[s * 40 + h * 2 + l] = cvtpk(res[0], res[1]);
    if (b == 0 && l == 0) __hip_atomic_store(ws + G_EPOCH, tag, RLX_AGENT);
    TS(11);
  }
}

static inline size_t ws_offset(size_t) { return 0; }

extern "C" void kernel_launch(void* const* d_in, const int* in_sizes, int n_in,
                              void* d_out, int out_size, void* d_ws, size_t ws_size,
                              hipStream_t stream) {
  Params p;
  p.x = (const unsigned short*)d_in[0];
  p.k = (const unsigned short*)d_in[1];
  p.v = (const unsigned short*)d_in[2];
  p.wq = (const unsigned short*)d_in[3];
  p.wo = (const unsigned short*)d_in[4];
  p.w1 = (const unsigned short*)d_in[6];
  p.b1 = (const unsigned short*)d_in[7];
  p.w2 = (const unsigned short*)d_in[8];
  p.b2 = (const unsigned short*)d_in[9];
  p.out = (unsigned short*)d_out;
  p.ws = (u64*)((char*)d_ws + ws_offset(ws_size));
  hipLaunchKernelGGL(fused_decode, dim3(NWG), dim3(NT), 0, stream, p);
}
```

```cpp
#include <hip/hip_runtime.h>
#include <hip/hip_bf16.h>
#include <stdint.h>

typedef unsigned v4u __attribute__((ext_vector_type(4)));
typedef float f32x4 __attribute__((ext_vector_type(4)));
typedef short bf16x8 __attribute__((ext_vector_type(8)));
typedef __bf16 bf16x2_t __attribute__((ext_vector_type(2)));
typedef unsigned long long u64;
#define GAS __attribute__((address_space(1)))
#define LAS __attribute__((address_space(3)))
#define RLX_AGENT __ATOMIC_RELAXED, __HIP_MEMORY_SCOPE_AGENT
#define RLX_WG __ATOMIC_RELAXED, __HIP_MEMORY_SCOPE_WORKGROUP
#define CAS __attribute__((address_space(4)))
typedef GAS u64 gu64;

#define WIN 2
#define WIN0 2
#define QI 4
#define GI 1
#define EI 9
#define WINE 4
#define FSKIP 0
#define MLEARLY 1
#define CODEPF 0
#define GSKIP 1
#define GSLEEP 0
#define FSLEEP 0
#define FDELAY 0
#define GDELAY 16
#define CODEPF2 0
#define NEARLY 16
#define LATESLEEP 4
#define DIAGACK 0
#define CATCH 0ull
#define WOIMM 16
#define PUBW 10
#define WOPOS 8
#define WOSPLIT 1
#define WOGATE 16
#define PUBPOS 5
constexpr int KV_AUX = 2, W_AUX = 2;

constexpr int DM = 640, HQ = 32, DH = 192, SK = 32768, WN = 4096, FF = 1536;
constexpr int NWG = 256, NT = 1024;
constexpr int ROWB = DH * 2;
constexpr int GRPB = 8 * ROWB;
constexpr float LOG2E = 1.4426950408889634f;
constexpr unsigned OOB = 0x40000000u;
constexpr int RSRC_W3 = 0x00020000;
constexpr unsigned SPIN_CAP = 1u << 18;

constexpr size_t G_EPOCH = 0;
constexpr size_t G_Q = 64;
constexpr size_t G_O = G_Q + 256 * 64;
constexpr size_t G_Y = G_O + (size_t)NWG * 128;
constexpr size_t G_END = G_Y + (size_t)NWG * 80;
constexpr size_t G_TS = G_END + 64;

constexpr int QP_STR = 68;
constexpr int OP_STR = 200;
constexpr int YP_STR = 164;
constexpr int L_QP = 0;
constexpr int L_OP = L_QP + 20 * QP_STR;
constexpr int L_YP = L_OP + 32 * OP_STR;
constexpr int L_X = L_YP + 16 * YP_STR;
constexpr int L_QB = L_X + 512;
constexpr int L_OB = L_QB + 96;
constexpr int L_HB = L_OB + 96;
constexpr int L_M = L_HB + 48;
constexpr int L_L = L_M + 16;
constexpr int L_FLAG = L_L + 16;
constexpr int L_END = L_FLAG + 8;

struct Params {
  const unsigned short* x;
  const unsigned short* k;
  const unsigned short* v;
  const unsigned short* wq;
  const unsigned short* wo;
  const unsigned short* w1;
  const unsigned short* b1;
  const unsigned short* w2;
  const unsigned short* b2;
  unsigned short* out;
  u64* ws;
};

__device__ __forceinline__ float bf2f(unsigned h) { return __builtin_bit_cast(float, h << 16); }
__device__ __forceinline__ unsigned cvtpk(float lo, float hi) {
  bf16x2_t r = {(__bf16)lo, (__bf16)hi};
  return __builtin_bit_cast(unsigned, r);
}
__device__ __forceinline__ float rbf(float f) { return bf2f(cvtpk(f, f) & 0xffffu); }
__device__ __forceinline__ float dot2(unsigned a, unsigned b, float acc) {
  return __builtin_amdgcn_fdot2_f32_bf16(__builtin_bit_cast(bf16x2_t, a), __builtin_bit_cast(bf16x2_t, b), acc, false);
}
template <int CTRL> __device__ __forceinline__ float dpp(float x) {
  return __builtin_bit_cast(float, __builtin_amdgcn_mov_dpp(__builtin_bit_cast(int, x), CTRL, 0xf, 0xf, true));
}
constexpr int XOR1 = 0xB1, XOR2 = 0x4E, HMIR = 0x141, ROR8 = 0x128;
__device__ __forceinline__ float sum8(float x) { x += dpp<XOR1>(x); x += dpp<XOR2>(x); x += dpp<HMIR>(x); return x; }
__device__ __forceinline__ float sum16(float x) { x = sum8(x); x += dpp<ROR8>(x); return x; }
__device__ __forceinline__ float max16(float x) {
  x = fmaxf(x, dpp<XOR1>(x)); x = fmaxf(x, dpp<XOR2>(x)); x = fmaxf(x, dpp<HMIR>(x)); x = fmaxf(x, dpp<ROR8>(x)); return x; }
__device__ __forceinline__ float xrow_sum(float x) {
  auto s = __builtin_amdgcn_permlane16_swap(__float_as_uint(x), __float_as_uint(x), false, false);
  x = __uint_as_float(s[0]) + __uint_as_float(s[1]);
  auto t = __builtin_amdgcn_permlane32_swap(__float_as_uint(x), __float_as_uint(x), false, false);
  return __uint_as_float(t[0]) + __uint_as_float(t[1]);
}
__device__ __forceinline__ float xrow_max(float x) {
  auto s = __builtin_amdgcn_permlane16_swap(__float_as_uint(x), __float_as_uint(x), false, false);
  x = fmaxf(__uint_as_float(s[0]), __uint_as_float(s[1]));
  auto t = __builtin_amdgcn_permlane32_swap(__float_as_uint(x), __float_as_uint(x), false, false);
  return fmaxf(__uint_as_float(t[0]), __uint_as_float(t[1]));
}
__device__ __forceinline__ f32x4 mfma(v4u a, v4u b, f32x4 c) {
  return __builtin_amdgcn_mfma_f32_16x16x32_bf16(__builtin_bit_cast(bf16x8, a), __builtin_bit_cast(bf16x8, b), c, 0, 0, 0);
}
__device__ __forceinline__ __amdgpu_buffer_rsrc_t mkrsrc(const void* p, unsigned bytes) {
  return __builtin_amdgcn_make_buffer_rsrc((void*)p, 0, bytes, RSRC_W3);
}
#define SB() __builtin_amdgcn_sched_barrier(0)
#define VMWAIT(n) asm volatile("s_waitcnt vmcnt(%0)" ::"n"(n) : "memory")
#define LGKM0() asm volatile("s_waitcnt lgkmcnt(0)" ::: "memory")
#define BAR() do { LGKM0(); __builtin_amdgcn_s_barrier(); asm volatile("" ::: "memory"); } while (0)

__device__ __forceinline__ void put_gran(gu64* g, float v, unsigned tlo, unsigned thi) {
  const unsigned hi = (__float_as_uint(v) & 0xffffff00u) | thi;
  __hip_atomic_store(g, ((u64)hi << 32) | tlo, RLX_AGENT);
}
__device__ __forceinline__ bool gran_ok(u64 g, unsigned tlo, unsigned thi) {
  return ((unsigned)g == tlo) & (((unsigned)(g >> 32) & 0xffu) == thi);
}
__device__ __forceinline__ float gran_val(u64 g) { return __uint_as_float((unsigned)(g >> 32) & 0xffffff00u); }
__device__ __forceinline__ void put_gran2(gu64* g, unsigned v, unsigned tlo) { __hip_atomic_store(g, ((u64)v << 32) | tlo, RLX_AGENT); }
__device__ __forceinline__ u64 get_gran(gu64* g) { return __hip_atomic_load(g, RLX_AGENT); }

__device__ __forceinline__ void lds_wait_ge(LAS unsigned* f, unsigned n) {
  while (__hip_atomic_load(f, RLX_WG) < n) __builtin_amdgcn_s_sleep(1);
  asm volatile("" ::: "memory");
}
__device__ __forceinline__ int opaque0() { int z; asm("v_mov_b32 %0, 0" : "=v"(z)); return z; }
__device__ __forceinline__ void lds_signal(LAS unsigned* f, int lane) {
  LGKM0();
  if (lane == 0) (void)__hip_atomic_fetch_add(f + opaque0(), 1u, RLX_WG);
}
__device__ __forceinline__ float qk_score(v4u k0, v4u k1, v4u k2, const v4u (&qf)[3]) {
  float d0 = dot2(k0.x, qf[0].x, 0.f), d1 = dot2(k0.y, qf[0].y, 0.f);
  d0 = dot2(k0.z, qf[0].z, d0); d1 = dot2(k0.w, qf[0].w, d1);
  d0 = dot2(k1.x, qf[1].x, d0); d1 = dot2(k1.y, qf[1].y, d1);
  d0 = dot2(k1.z, qf[1].z, d0); d1 = dot2(k1.w, qf[1].w, d1);
  d0 = dot2(k2.x, qf[2].x, d0); d1 = dot2(k2.y, qf[2].y, d1);
  d0 = dot2(k2.z, qf[2].z, d0); d1 = dot2(k2.w, qf[2].w, d1);
  return rbf(rbf(sum8(d0 + d1)) * 0.07216878364870322f);
}
#define KLOAD(i) __builtin_amdgcn_raw_buffer_load_b128(rk, voff + ((i) % 3) * 128, woff + ((i) / 3) * GRPB, KV_AUX)
#define VLOAD(i) __builtin_amdgcn_raw_buffer_load_b128(rv, voff + ((i) % 3) * 128, woff + ((i) / 3) * GRPB, KV_AUX)

#define TS(i) do {} while (0)

__global__ void __launch_bounds__(NT) fused_decode(Params p) {
  __shared__ __attribute__((aligned(16))) float lds_[L_END];
  LAS float* lds = (LAS float*)lds_;
  LAS unsigned* flags = (LAS unsigned*)(lds + L_FLAG);
  const int tid = threadIdx.x, l = tid & 63, w = __builtin_amdgcn_readfirstlane(tid >> 6);
  const int b = blockIdx.x;
#define XMAP 1
  const int s2 = (b >> 3) & 7, h2 = (b & 7) * 4 + (b >> 6);
  const int s = XMAP ? (b & 7) : s2, h = XMAP ? (b >> 3) : h2;
  TS(0);
  gu64* ws = (gu64*)p.ws;

  const unsigned voff = (unsigned)((l >> 3) * ROWB + (l & 7) * 16);
  const size_t kvoff = ((size_t)h * SK + (SK - WN) + (size_t)s * (WN / 8)) * DH;
  const __amdgpu_buffer_rsrc_t rk = mkrsrc(p.k + kvoff, (WN / 8) * ROWB);
  const __amdgpu_buffer_rsrc_t rv = mkrsrc(p.v + kvoff, (WN / 8) * ROWB);
  const int woff = w * (4 * GRPB);

  const bool proj = w < 10 && b < 240;
  const int row0 = (b & 1) * 320 + 32 * w;
  v4u K[12], V[12], A[4], X0, X1;
  if (__builtin_expect(proj, 1)) {
    const int ld = b < 192 ? HQ * DH : FF;
    const unsigned short* wb = b < 192 ? p.wq + (b >> 1) * 64 : p.w1 + ((b - 192) >> 1) * 64;
    const __amdgpu_buffer_rsrc_t ra = mkrsrc(wb + (size_t)row0 * ld, 32 * ld * 2);
    const __amdgpu_buffer_rsrc_t rx = mkrsrc(p.x, DM * 2);
    const unsigned aoff = (unsigned)((l >> 3) * ld * 2 + (l & 7) * 16);
    X0 = __builtin_amdgcn_raw_buffer_load_b128(rx, l * 16, 0, 0);
    X1 = __builtin_amdgcn_raw_buffer_load_b128(rx, l * 16 + 1024, 0, 0);
#pragma unroll
    for (int i = 0; i < 4; ++i) A[i] = __builtin_amdgcn_raw_buffer_load_b128(ra, aoff, i * 8 * ld * 2, W_AUX);
  }
  SB();
#pragma unroll
  for (int i = 0; i < WIN0; ++i) K[i] = KLOAD(i);
  SB();
  if (tid < 8) flags[tid] = 0;
  TS(12);
  BAR();
  TS(13);
  const u64 epv = get_gran(ws + G_EPOCH);
  SB();

  v4u mk;
  {
    const unsigned hm = (l & 1) ? 0xffff0000u : 0x0000ffffu;
    const int q4 = (l & 7) >> 1;
    mk.x = q4 == 0 ? hm : 0u; mk.y = q4 == 1 ? hm : 0u; mk.z = q4 == 2 ? hm : 0u; mk.w = q4 == 3 ? hm : 0u;
  }
  const bool valid = ((l >> 3) & 1) == (l >> 5);

  if (proj) {
    *(LAS v4u*)(lds + L_X + 4 * l) = X0;
    *(LAS v4u*)(lds + L_X + 256 + 4 * l) = X1;
    unsigned xr[4];
#pragma unroll
    for (int i = 0; i < 4; ++i) xr[i] = ((LAS unsigned short*)(lds + L_X))[row0 + 8 * i + (l >> 3)];
    float xs[16], sum = 0.f;
#pragma unroll
    for (int i = 0; i < 4; ++i) {
      xs[2 * i] = bf2f(X0[i] & 0xffffu); xs[2 * i + 1] = __uint_as_float(X0[i] & 0xffff0000u);
      xs[8 + 2 * i] = bf2f(X1[i] & 0xffffu); xs[8 + 2 * i + 1] = __uint_as_float(X1[i] & 0xffff0000u);
    }
#pragma unroll
    for (int i = 0; i < 8; ++i) sum += xs[i] + xs[8 + i];
    sum = xrow_sum(sum16(sum));
    const float mean = sum * (1.0f / DM);
    float sq = 0.f, sq1 = 0.f;
#pragma unroll
    for (int i = 0; i < 8; ++i) { const float d = xs[i] - mean, e = xs[8 + i] - mean; sq = fmaf(d, d, sq); sq1 = fmaf(e, e, sq1); }
    sq += l < 16 ? sq1 : 0.f;
    sq = xrow_sum(sum16(sq));
    const float sd = rbf(sqrtf(rbf(rbf(sq * (1.0f / DM)) + 1e-5f)));
    const float mb = rbf(mean);
    f32x4 d = {0.f, 0.f, 0.f, 0.f};
#pragma unroll
    for (int i = 0; i < 4; ++i) {
      const float xn = rbf(bf2f(xr[i]) - mb) / sd;
      d = mfma(mk & cvtpk(xn, xn), A[i], d);
    }
    if (valid) *(LAS f32x4*)(lds + L_QP + (2 * w + (l >> 5)) * QP_STR + 8 * (l & 7) + 4 * ((l >> 4) & 1)) = d;
    lds_signal(flags + 0, l);
    TS(1);
  }

  u64 tag;
  const int catchup = WIN0 + (int)((unsigned long long)CATCH >> (4 * w) & 15);
#pragma unroll
  for (int i = WIN0; i < 12; ++i) {
    if (((i == PUBPOS + 1 || i == PUBPOS + 2) && w == PUBW) || i < catchup) VMWAIT(WIN);
    else VMWAIT(WIN - 1);
    K[i] = KLOAD(i);
    SB();
    if (i == PUBPOS) {
      tag = ((u64)__builtin_amdgcn_readfirstlane((unsigned)(epv >> 32)) << 32 | (u64)__builtin_amdgcn_readfirstlane((unsigned)epv)) + 1;
      if (w == PUBW && b < 240) {
        lds_wait_ge(flags + 0, 10);
        float acc = 0.f;
#pragma unroll
        for (int u = 0; u < 20; ++u) acc += lds[L_QP + u * QP_STR + l];
        put_gran(ws + G_Q + b * 64 + l, acc, (unsigned)tag, (unsigned)(tag >> 32) & 0xffu);
        TS(2);
      }
      SB();
    }
  }
  const unsigned tlo = (unsigned)tag, thi = (unsigned)(tag >> 32) & 0xffu;

  u64 g0 = 0, g1 = 0;
  unsigned b1v = 0;
  const int qcol = w < 15 ? h * DH + (w - 12) * 64 + l : HQ * DH + h2 * 48 + (l < 48 ? l : 0);
  gu64* qsrc = ws + G_Q + (qcol >> 6) * 128 + (qcol & 63);
  if (w >= 12) {
    g0 = get_gran(qsrc); g1 = get_gran(qsrc + 64);
    b1v = ((const GAS unsigned*)p.b1)[h2 * 24 + (l < 48 ? l >> 1 : 0)];
  }
  SB();

  float sc[4], mwg;
  v4u qf[3], sel[4], WO[3], W2;
  f32x4 acc[3] = {{0.f, 0.f, 0.f, 0.f}, {0.f, 0.f, 0.f, 0.f}, {0.f, 0.f, 0.f, 0.f}};
  unsigned xfin = 0, b2fin = 0;
  const __amdgpu_buffer_rsrc_t ro = mkrsrc(p.wo + (size_t)(h2 * DH + 12 * w) * DM + s2 * 80, 12 * DM * 2);
  const unsigned oo = (l & 15) < 10 ? (unsigned)((l >> 4) * (DM * 2) + (l & 15) * 16) : OOB;
  auto load_wo = [&](int lo, int hi, bool paced) {
#pragma unroll
    for (int u = lo; u < hi; ++u) {
      if (paced) VMWAIT(WIN - 1);
      if (u == 0) {
        const __amdgpu_buffer_rsrc_t r2 = mkrsrc(p.w2 + (size_t)(h2 * 48 + 4 * (w < 12 ? w : 0)) * DM + s2 * 80, w < 12 ? 4 * DM * 2 : 0);
        W2 = __builtin_amdgcn_raw_buffer_load_b128(r2, oo, 0, W_AUX);
      } else {
        WO[u - 1] = __builtin_amdgcn_raw_buffer_load_b128(ro, oo, (u - 1) * 4 * DM * 2, W_AUX);
      }
      SB();
    }
  };
#pragma unroll
  for (int i = 0; i < 12; ++i) {
    if (i >= EI) VMWAIT(WINE - 1);
    else if (i < WIN && w >= 12) VMWAIT(WIN + 2);
    else VMWAIT(WIN - 1);
    V[i] = VLOAD(i);
    SB();
    if (i == GI && w >= 12) {
      if (__builtin_expect(!__all(gran_ok(g0, tlo, thi) & gran_ok(g1, tlo, thi)), 0)) {
        for (unsigned spins = 0; spins < SPIN_CAP; ++spins) {
          __builtin_amdgcn_s_sleep(4);
          g0 = get_gran(qsrc); g1 = get_gran(qsrc + 64);
          if (__all(gran_ok(g0, tlo, thi) & gran_ok(g1, tlo, thi))) break;
        }
      }
      const float tot = gran_val(g0) + gran_val(g1);
      if (w < 15) {
        ((LAS unsigned short*)(lds + L_QB))[(w - 12) * 64 + l] = (unsigned short)(cvtpk(tot, tot) & 0xffffu);
      } else {
        float hv = rbf(rbf(tot) + ((l & 1) ? __uint_as_float(b1v & 0xffff0000u) : bf2f(b1v & 0xffffu)));
        hv = hv > 0.f ? hv : 0.f;
        hv = hv * hv;
        if (l < 48) ((LAS unsigned*)(lds + L_HB))[l] = cvtpk(hv, hv);
      }
      lds_signal(flags + 1, l);
      TS(3);
    }
    if (i == QI) {
      TS(4);
      lds_wait_ge(flags + 1, 4);
#pragma unroll
      for (int j = 0; j < 3; ++j) qf[j] = *(LAS v4u*)(lds + L_QB + 32 * j + 4 * (l & 7));
      TS(5);
      sc[0] = qk_score(K[0], K[1], K[2], qf);
      sc[1] = qk_score(K[3], K[4], K[5], qf);
    }
    if (i == QI + 1) {
      sc[2] = qk_score(K[6], K[7], K[8], qf);
      sc[3] = qk_score(K[9], K[10], K[11], qf);
      float mw = fmaxf(fmaxf(sc[0], sc[1]), fmaxf(sc[2], sc[3]));
      mw = xrow_max(fmaxf(mw, dpp<ROR8>(mw)));
      if (l == 0) lds[L_M + w] = mw;
      lds_signal(flags + 2, l);
    }
    if (i == QI + 3) {
      lds_wait_ge(flags + 2, 16);
      mwg = max16(lds[L_M + (l & 15)]);
      float lw = 0.f;
#pragma unroll
      for (int g = 0; g < 4; ++g) {
        const float pr = __builtin_amdgcn_exp2f((sc[g] - mwg) * LOG2E);
        lw += pr;
        sel[g] = mk & cvtpk(pr, pr);
      }
      lw = xrow_sum(lw + dpp<ROR8>(lw));
      if (l == 0) lds[L_L + w] = lw;
      if (MLEARLY) lds_signal(flags + 4, l);
#pragma unroll
      for (int u = 0; u < 6; ++u) acc[u % 3] = mfma(sel[u / 3], V[u], acc[u % 3]);
      TS(6);
    }
    if (i == 10) {
#pragma unroll
      for (int u = 6; u < 9; ++u) acc[u % 3] = mfma(sel[2], V[u], acc[u % 3]);
    }
    SB();
    if (i == WOPOS) load_wo(0, WOSPLIT, true);
    if (NEARLY < 16 && i == WOPOS && (w >= 14 || (DIAGACK && w == 0))) load_wo(WOSPLIT, 4, false);
    if (MLEARLY && i == QI + 5 && w == 11) {
      lds_wait_ge(flags + 4, 16);
      const float lsum = sum16(lds[L_L + (l & 15)]);
      if (l < 16) put_gran(ws + G_O + (size_t)(h * 8 + s) * 128 + 96 + l, l == 0 ? mwg : lsum, tlo, thi);
      SB();
    }
  }
  if (WOSPLIT < 4) lds_signal(flags + 3, l);
  if (WOIMM) {
    lds_wait_ge(flags + 3, WOIMM);
    load_wo(WOSPLIT, 4, false);
  }
#pragma unroll
  for (int u = 9; u < 12; ++u) acc[u % 3] = mfma(sel[3], V[u], acc[u % 3]);
  {
    LAS float* op = lds + L_OP + (w + 16 * (l >> 5)) * OP_STR + 8 * (l & 7) + 4 * ((l >> 4) & 1);
#pragma unroll
    for (int j = 0; j < 3; ++j) if (valid) *(LAS f32x4*)(op + 64 * j) = acc[j];
  }
  TS(7);
  bool late = false;
  if (NEARLY < 16) {
    if (w < 14 && !(DIAGACK && w == 0)) {
      unsigned arr = 0;
      if (l == 0) arr = __hip_atomic_fetch_add(flags + 6 + opaque0(), 1u, RLX_WG);
      late = __builtin_amdgcn_readfirstlane(arr) >= NEARLY;
      if (!late) {
        lds_wait_ge(flags + 3, WOGATE);
        load_wo(WOSPLIT, 4, false);
      }
    }
  } else if (WOSPLIT < 4 && !WOIMM) {
    lds_wait_ge(flags + 3, WOGATE);
    load_wo(WOSPLIT, 4, false);
  }
  TS(15);
  BAR();
  gu64* og = ws + G_O + (size_t)(h2 * 8) * 128;
  gu64* op_ = ws + G_O + (size_t)(h * 8 + s) * 128;
  if (w < 6) {
    const int c4 = tid >> 3, pg = tid & 7;
    LAS float* src = lds + L_OP + pg * OP_STR + 4 * c4;
    f32x4 t = (*(LAS f32x4*)(src) + *(LAS f32x4*)(src + 8 * OP_STR)) + (*(LAS f32x4*)(src + 16 * OP_STR) + *(LAS f32x4*)(src + 24 * OP_STR));
#pragma unroll
    for (int r = 0; r < 4; ++r) t[r] = sum8(t[r]);
    const unsigned pk = (pg & 1) ? cvtpk(t[2], t[3]) : cvtpk(t[0], t[1]);
    if (pg < 2) put_gran2(op_ + 2 * c4 + pg, pk, tlo);
  } else if (!MLEARLY && w == 6) {
    const float lsum = sum16(lds[L_L + (l & 15)]);
    if (l < 16) put_gran(op_ + 96 + l, l == 0 ? mwg : lsum, tlo, thi);
  }
  TS(8);
  if (DIAGACK && w == 0) { VMWAIT(0); TS(1); }
  if (late) {
    if (w >= 6 && LATESLEEP) __builtin_amdgcn_s_sleep(LATESLEEP);
    load_wo(WOSPLIT, 4, false);
  }
  if (w == 10 && h2 < 20) {
    xfin = ((const GAS unsigned*)p.x)[s2 * 40 + h2 * 2 + (l & 1)];
    b2fin = ((const GAS unsigned*)p.b2)[s2 * 40 + h2 * 2 + (l & 1)];
  }
  if (w >= 14) {
    const int d = (w - 14) * 48 + (l < 48 ? l : 0);
    u64 g[8], gm;
    unsigned miss = 0x1ffu;
    float f = 0.f;
    if (GDELAY) __builtin_amdgcn_s_sleep(GDELAY);
    for (unsigned spins = 0;; ++spins) {
#pragma unroll
      for (int i = 0; i < 8; ++i) if (miss >> i & 1u) g[i] = get_gran(og + i * 128 + d);
      unsigned m2 = 0;
      if (miss >> 8) {
        gm = get_gran(og + ((l >> 1) & 7) * 128 + 96 + (l & 1));
        if (__all(gran_ok(gm, tlo, thi))) {
          const float ml = gran_val(gm);
          const float mo = dpp<XOR1>(ml);
          const float ms = (l & 1) ? mo : ml, ls = (l & 1) ? ml : mo;
          const float M = max16(ms);
          const float e = __builtin_amdgcn_exp2f((ms - M) * LOG2E);
          f = e * __builtin_amdgcn_rcpf(0.5f * sum16(e * ls));
        } else {
          m2 = 0x100u;
        }
      }
#pragma unroll
      for (int i = 0; i < 8; ++i) m2 |= __all((unsigned)g[i] == tlo) ? 0u : 1u << i;
      miss = m2;
      if (miss == 0 || spins > SPIN_CAP) break;
      if (!GSKIP) miss |= 0xffu;
      if (GSLEEP) __builtin_amdgcn_s_sleep(GSLEEP);
    }
    float o0 = 0.f, o1 = 0.f;
#pragma unroll
    for (int i = 0; i < 8; ++i) {
      const float fi = __builtin_bit_cast(float, __builtin_amdgcn_readlane(__builtin_bit_cast(int, f), 2 * i));
      const unsigned v = (unsigned)(g[i] >> 32);
      o0 = fmaf(fi, bf2f(v & 0xffffu), o0);
      o1 = fmaf(fi, __uint_as_float(v & 0xffff0000u), o1);
    }
    if (l < 48) ((LAS unsigned*)(lds + L_OB))[d] = cvtpk(o0, o1);
  }
  const unsigned lm = (l & 8) ? 0u : 0xffffffffu;
  LAS float* yp = lds + L_YP + w * YP_STR + 8 * (l & 15) + 4 * (l >> 4);
  const bool yl = (l & 15) < 10 && l < 32;
  const int yc = (w & 1) * 64 + l;
  float zs = 0.f;
  if (w < 12) {
    const unsigned hd = ((LAS unsigned*)(lds + L_HB))[4 * w + (l >> 4)];
    const f32x4 z = mfma(mk & (hd & lm), W2, f32x4{0.f, 0.f, 0.f, 0.f});
    if (yl) *(LAS f32x4*)(yp + 80) = z;
    lds_signal(flags + 5, l);
    if (w < 2) {
      lds_wait_ge(flags + 5, 12);
      if (yc < 80) {
#pragma unroll
        for (int u = 0; u < 12; ++u) zs += lds[L_YP + u * YP_STR + 80 + yc];
      }
      asm volatile("" : "+v"(zs));
    }
  }
  if (DIAGACK && w > 0 && w < 14) { VMWAIT(0); TS(2); }
  TS(9);
  BAR();
  {
    f32x4 y[3];
#pragma unroll
    for (int i = 0; i < 3; ++i) {
      const unsigned ov = ((LAS unsigned short*)(lds + L_OB))[12 * w + 4 * i + (l >> 4)];
      y[i] = mfma(mk & ((ov | (ov << 16)) & lm), WO[i], f32x4{0.f, 0.f, 0.f, 0.f});
    }
    if (yl) *(LAS f32x4*)(yp) = (y[0] + y[1]) + y[2];
  }
  BAR();
  if (w < 2) {
    if (yc < 80) {
      float t[16];
#pragma unroll
      for (int u = 0; u < 16; ++u) t[u] = lds[L_YP + u * YP_STR + yc];
#pragma unroll
      for (int st = 8; st > 0; st >>= 1)
#pragma unroll
        for (int u = 0; u < st; ++u) t[u] += t[u + st];
      put_gran2(ws + G_Y + (size_t)(s2 * 32 + h2) * 80 + yc, cvtpk(t[0], zs), tlo);
    }
    TS(10);
    if (DIAGACK) { VMWAIT(0); TS(3); }
  } else if (w == 10 && h2 < 20) {
    gu64* ys = ws + G_Y + (size_t)(s2 * 32 + (l >> 1)) * 80 + 4 * h2 + 2 * (l & 1);
    u64 y0 = 0, y1 = 0;
    bool ok = false;
    if (FDELAY) __builtin_amdgcn_s_sleep(FDELAY);
    for (unsigned spins = 0;; ++spins) {
      if (!ok || !FSKIP) { y0 = get_gran(ys); y1 = get_gran(ys + 1); }
      ok = ((unsigned)y0 == tlo) & ((unsigned)y1 == tlo);
      if (__all(ok) || spins > SPIN_CAP) break;
      if (FSLEEP) __builtin_amdgcn_s_sleep(FSLEEP);
    }
    const unsigned p0 = (unsigned)(y0 >> 32), p1 = (unsigned)(y1 >> 32);
    float v4[4] = {bf2f(p0 & 0xffffu), bf2f(p1 & 0xffffu), __uint_as_float(p0 & 0xffff0000u), __uint_as_float(p1 & 0xffff0000u)};
#pragma unroll
    for (int r = 0; r < 4; ++r) {
      float t = v4[r];
      t += dpp<XOR2>(t);
      const float u = dpp<HMIR>(t);
      t += dpp<XOR1>(u);
      t += dpp<ROR8>(t);
      v4[r] = xrow_sum(t);
    }
    float res[2];
#pragma unroll
    for (int r = 0; r < 2; ++r) {
      const float sv = rbf(v4[r]);
      const float bb = r == 0 ? bf2f(b2fin & 0xffffu) : __uint_as_float(b2fin & 0xffff0000u);
      const float xx = r == 0 ? bf2f(xfin & 0xffffu) : __uint_as_float(xfin & 0xffff0000u);
      const float o2 = rbf(rbf(v4[2 + r]) + bb);
      const float gt = rbf(o2 * __builtin_amdgcn_rcpf(1.0f + __builtin_amdgcn_exp2f(-o2 * LOG2E)));
      res[r] = xx + rbf(sv * gt);
    }
    if (l < 2) ((GAS unsigned*)p.out)[s2 * 40 + h2 * 2 + l] = cvtpk(res[0], res[1]);
    if (b == 0 && l == 0) __hip_atomic_store(ws + G_EPOCH, tag, RLX_AGENT);
    TS(11);
  }
}

static inline size_t ws_offset(size_t) { return 0; }

extern "C" void kernel_launch(void* const* d_in, const int* in_sizes, int n_in,
                              void* d_out, int out_size, void* d_ws, size_t ws_size,
                              hipStream_t stream) {
  Params p;
  p.x = (const unsigned short*)d_in[0];
  p.k = (const unsigned short*)d_in[1];
  p.v = (const unsigned short*)d_in[2];
  p.wq = (const unsigned short*)d_in[3];
  p.wo = (const unsigned short*)d_in[4];
  p.w1 = (const unsigned short*)d_in[6];
  p.b1 = (const unsigned short*)d_in[7];
  p.w2 = (const unsigned short*)d_in[8];
  p.b2 = (const unsigned short*)d_in[9];
  p.out = (unsigned short*)d_out;
  p.ws = (u64*)((char*)d_ws + ws_offset(ws_size));
  hipLaunchKernelGGL(fused_decode, dim3(NWG), dim3(NT), 0, stream, p);
}
```

```cpp
#include <hip/hip_runtime.h>
#include <hip/hip_bf16.h>
#include <stdint.h>

typedef unsigned v4u __attribute__((ext_vector_type(4)));
typedef float f32x4 __attribute__((ext_vector_type(4)));
typedef short bf16x8 __attribute__((ext_vector_type(8)));
typedef __bf16 bf16x2_t __attribute__((ext_vector_type(2)));
typedef unsigned long long u64;
#define GAS __attribute__((address_space(1)))
#define LAS __attribute__((address_space(3)))
#define RLX_AGENT __ATOMIC_RELAXED, __HIP_MEMORY_SCOPE_AGENT
#define RLX_WG __ATOMIC_RELAXED, __HIP_MEMORY_SCOPE_WORKGROUP
#define CAS __attribute__((address_space(4)))
typedef GAS u64 gu64;

#define WIN 2
#define WIN0 2
#define QI 4
#define GI 1
#define EI 9
#define WINE 4
#define FSKIP 0
#define MLEARLY 1
#define CODEPF 0
#define GSKIP 1
#define GSLEEP 0
#define FSLEEP 0
#define FDELAY 0
#define GDELAY 16
#define CODEPF2 0
#define NEARLY 16
#define LATESLEEP 4
#define DIAGACK 0
#define CATCH 0ull
#define WOIMM 0
#define PACEA 130
#define PACEB 0
#define WINB 3
#define PUBW 10
#define WOPOS 8
#define WOSPLIT 1
#define WOGATE 16
#define PUBPOS 5
constexpr int KV_AUX = 2, W_AUX = 2;

constexpr int DM = 640, HQ = 32, DH = 192, SK = 32768, WN = 4096, FF = 1536;
constexpr int NWG = 256, NT = 1024;
constexpr int ROWB = DH * 2;
constexpr int GRPB = 8 * ROWB;
constexpr float LOG2E = 1.4426950408889634f;
constexpr unsigned OOB = 0x40000000u;
constexpr int RSRC_W3 = 0x00020000;
constexpr unsigned SPIN_CAP = 1u << 18;

constexpr size_t G_EPOCH = 0;
constexpr size_t G_Q = 64;
constexpr size_t G_O = G_Q + 256 * 64;
constexpr size_t G_Y = G_O + (size_t)NWG * 128;
constexpr size_t G_END = G_Y + (size_t)NWG * 80;
constexpr size_t G_TS = G_END + 64;

constexpr int QP_STR = 68;
constexpr int OP_STR = 200;
constexpr int YP_STR = 164;
constexpr int L_QP = 0;
constexpr int L_OP = L_QP + 20 * QP_STR;
constexpr int L_YP = L_OP + 32 * OP_STR;
constexpr int L_X = L_YP + 16 * YP_STR;
constexpr int L_QB = L_X + 512;
constexpr int L_OB = L_QB + 96;
constexpr int L_HB = L_OB + 96;
constexpr int L_M = L_HB + 48;
constexpr int L_L = L_M + 16;
constexpr int L_FLAG = L_L + 16;
constexpr int L_END = L_FLAG + 8;

struct Params {
  const unsigned short* x;
  const unsigned short* k;
  const unsigned short* v;
  const unsigned short* wq;
  const unsigned short* wo;
  const unsigned short* w1;
  const unsigned short* b1;
  const unsigned short* w2;
  const unsigned short* b2;
  unsigned short* out;
  u64* ws;
};

__device__ __forceinline__ float bf2f(unsigned h) { return __builtin_bit_cast(float, h << 16); }
__device__ __forceinline__ unsigned cvtpk(float lo, float hi) {
  bf16x2_t r = {(__bf16)lo, (__bf16)hi};
  return __builtin_bit_cast(unsigned, r);
}
__device__ __forceinline__ float rbf(float f) { return bf2f(cvtpk(f, f) & 0xffffu); }
__device__ __forceinline__ float dot2(unsigned a, unsigned b, float acc) {
  return __builtin_amdgcn_fdot2_f32_bf16(__builtin_bit_cast(bf16x2_t, a), __builtin_bit_cast(bf16x2_t, b), acc, false);
}
template <int CTRL> __device__ __forceinline__ float dpp(float x) {
  return __builtin_bit_cast(float, __builtin_amdgcn_mov_dpp(__builtin_bit_cast(int, x), CTRL, 0xf, 0xf, true));
}
constexpr int XOR1 = 0xB1, XOR2 = 0x4E, HMIR = 0x141, ROR8 = 0x128;
__device__ __forceinline__ float sum8(float x) { x += dpp<XOR1>(x); x += dpp<XOR2>(x); x += dpp<HMIR>(x); return x; }
__device__ __forceinline__ float sum16(float x) { x = sum8(x); x += dpp<ROR8>(x); return x; }
__device__ __forceinline__ float max16(float x) {
  x = fmaxf(x, dpp<XOR1>(x)); x = fmaxf(x, dpp<XOR2>(x)); x = fmaxf(x, dpp<HMIR>(x)); x = fmaxf(x, dpp<ROR8>(x)); return x; }
__device__ __forceinline__ float xrow_sum(float x) {
  auto s = __builtin_amdgcn_permlane16_swap(__float_as_uint(x), __float_as_uint(x), false, false);
  x = __uint_as_float(s[0]) + __uint_as_float(s[1]);
  auto t = __builtin_amdgcn_permlane32_swap(__float_as_uint(x), __float_as_uint(x), false, false);
  return __uint_as_float(t[0]) + __uint_as_float(t[1]);
}
__device__ __forceinline__ float xrow_max(float x) {
  auto s = __builtin_amdgcn_permlane16_swap(__float_as_uint(x), __float_as_uint(x), false, false);
  x = fmaxf(__uint_as_float(s[0]), __uint_as_float(s[1]));
  auto t = __builtin_amdgcn_permlane32_swap(__float_as_uint(x), __float_as_uint(x), false, false);
  return fmaxf(__uint_as_float(t[0]), __uint_as_float(t[1]));
}
__device__ __forceinline__ f32x4 mfma(v4u a, v4u b, f32x4 c) {
  return __builtin_amdgcn_mfma_f32_16x16x32_bf16(__builtin_bit_cast(bf16x8, a), __builtin_bit_cast(bf16x8, b), c, 0, 0, 0);
}
__device__ __forceinline__ __amdgpu_buffer_rsrc_t mkrsrc(const void* p, unsigned bytes) {
  return __builtin_amdgcn_make_buffer_rsrc((void*)p, 0, bytes, RSRC_W3);
}
#define SB() __builtin_amdgcn_sched_barrier(0)
#define VMWAIT(n) asm volatile("s_waitcnt vmcnt(%0)" ::"n"(n) : "memory")
#define LGKM0() asm volatile("s_waitcnt lgkmcnt(0)" ::: "memory")
#define BAR() do { LGKM0(); __builtin_amdgcn_s_barrier(); asm volatile("" ::: "memory"); } while (0)

__device__ __forceinline__ void put_gran(gu64* g, float v, unsigned tlo, unsigned thi) {
  const unsigned hi = (__float_as_uint(v) & 0xffffff00u) | thi;
  __hip_atomic_store(g, ((u64)hi << 32) | tlo, RLX_AGENT);
}
__device__ __forceinline__ bool gran_ok(u64 g, unsigned tlo, unsigned thi) {
  return ((unsigned)g == tlo) & (((unsigned)(g >> 32) & 0xffu) == thi);
}
__device__ __forceinline__ float gran_val(u64 g) { return __uint_as_float((unsigned)(g >> 32) & 0xffffff00u); }
__device__ __forceinline__ void put_gran2(gu64* g, unsigned v, unsigned tlo) { __hip_atomic_store(g, ((u64)v << 32) | tlo, RLX_AGENT); }
__device__ __forceinline__ u64 get_gran(gu64* g) { return __hip_atomic_load(g, RLX_AGENT); }

__device__ __forceinline__ void lds_wait_ge(LAS unsigned* f, unsigned n) {
  while (__hip_atomic_load(f, RLX_WG) < n) __builtin_amdgcn_s_sleep(1);
  asm volatile("" ::: "memory");
}
__device__ __forceinline__ int opaque0() { int z; asm("v_mov_b32 %0, 0" : "=v"(z)); return z; }
__device__ __forceinline__ void lds_signal(LAS unsigned* f, int lane) {
  LGKM0();
  if (lane == 0) (void)__hip_atomic_fetch_add(f + opaque0(), 1u, RLX_WG);
}
__device__ __forceinline__ float qk_score(v4u k0, v4u k1, v4u k2, const v4u (&qf)[3]) {
  float d0 = dot2(k0.x, qf[0].x, 0.f), d1 = dot2(k0.y, qf[0].y, 0.f);
  d0 = dot2(k0.z, qf[0].z, d0); d1 = dot2(k0.w, qf[0].w, d1);
  d0 = dot2(k1.x, qf[1].x, d0); d1 = dot2(k1.y, qf[1].y, d1);
  d0 = dot2(k1.z, qf[1].z, d0); d1 = dot2(k1.w, qf[1].w, d1);
  d0 = dot2(k2.x, qf[2].x, d0); d1 = dot2(k2.y, qf[2].y, d1);
  d0 = dot2(k2.z, qf[2].z, d0); d1 = dot2(k2.w, qf[2].w, d1);
  return rbf(rbf(sum8(d0 + d1)) * 0.07216878364870322f);
}
#define PACE(j) VMWAIT(WIN - 1)
#define KLOAD(i) __builtin_amdgcn_raw_buffer_load_b128(rk, voff + ((i) % 3) * 128, woff + ((i) / 3) * GRPB, KV_AUX)
#define VLOAD(i) __builtin_amdgcn_raw_buffer_load_b128(rv, voff + ((i) % 3) * 128, woff + ((i) / 3) * GRPB, KV_AUX)

#define TS(i) do {} while (0)

__global__ void __launch_bounds__(NT) fused_decode(Params p) {
  __shared__ __attribute__((aligned(16))) float lds_[L_END];
  LAS float* lds = (LAS float*)lds_;
  LAS unsigned* flags = (LAS unsigned*)(lds + L_FLAG);
  const int tid = threadIdx.x, l = tid & 63, w = __builtin_amdgcn_readfirstlane(tid >> 6);
  const int b = blockIdx.x;
#define XMAP 1
  const int s2 = (b >> 3) & 7, h2 = (b & 7) * 4 + (b >> 6);
  const int s = XMAP ? (b & 7) : s2, h = XMAP ? (b >> 3) : h2;
  TS(0);
  gu64* ws = (gu64*)p.ws;

  const unsigned voff = (unsigned)((l >> 3) * ROWB + (l & 7) * 16);
  const size_t kvoff = ((size_t)h * SK + (SK - WN) + (size_t)s * (WN / 8)) * DH;
  const __amdgpu_buffer_rsrc_t rk = mkrsrc(p.k + kvoff, (WN / 8) * ROWB);
  const __amdgpu_buffer_rsrc_t rv = mkrsrc(p.v + kvoff, (WN / 8) * ROWB);
  const int woff = w * (4 * GRPB);

  const bool proj = w < 10 && b < 240;
  const int row0 = (b & 1) * 320 + 32 * w;
  v4u K[12], V[12], A[4], X0, X1;
  if (__builtin_expect(proj, 1)) {
    const int ld = b < 192 ? HQ * DH : FF;
    const unsigned short* wb = b < 192 ? p.wq + (b >> 1) * 64 : p.w1 + ((b - 192) >> 1) * 64;
    const __amdgpu_buffer_rsrc_t ra = mkrsrc(wb + (size_t)row0 * ld, 32 * ld * 2);
    const __amdgpu_buffer_rsrc_t rx = mkrsrc(p.x, DM * 2);
    const unsigned aoff = (unsigned)((l >> 3) * ld * 2 + (l & 7) * 16);
    X0 = __builtin_amdgcn_raw_buffer_load_b128(rx, l * 16, 0, 0);
    X1 = __builtin_amdgcn_raw_buffer_load_b128(rx, l * 16 + 1024, 0, 0);
#pragma unroll
    for (int i = 0; i < 4; ++i) A[i] = __builtin_amdgcn_raw_buffer_load_b128(ra, aoff, i * 8 * ld * 2, W_AUX);
  }
  SB();
#pragma unroll
  for (int i = 0; i < WIN0; ++i) K[i] = KLOAD(i);
  SB();
  if (tid < 8) flags[tid] = 0;
  TS(12);
  BAR();
  TS(13);
  const u64 epv = get_gran(ws + G_EPOCH);
  SB();

  v4u mk;
  {
    const unsigned hm = (l & 1) ? 0xffff0000u : 0x0000ffffu;
    const int q4 = (l & 7) >> 1;
    mk.x = q4 == 0 ? hm : 0u; mk.y = q4 == 1 ? hm : 0u; mk.z = q4 == 2 ? hm : 0u; mk.w = q4 == 3 ? hm : 0u;
  }
  const bool valid = ((l >> 3) & 1) == (l >> 5);

  if (proj) {
    *(LAS v4u*)(lds + L_X + 4 * l) = X0;
    *(LAS v4u*)(lds + L_X + 256 + 4 * l) = X1;
    unsigned xr[4];
#pragma unroll
    for (int i = 0; i < 4; ++i) xr[i] = ((LAS unsigned short*)(lds + L_X))[row0 + 8 * i + (l >> 3)];
    float xs[16], sum = 0.f;
#pragma unroll
    for (int i = 0; i < 4; ++i) {
      xs[2 * i] = bf2f(X0[i] & 0xffffu); xs[2 * i + 1] = __uint_as_float(X0[i] & 0xffff0000u);
      xs[8 + 2 * i] = bf2f(X1[i] & 0xffffu); xs[8 + 2 * i + 1] = __uint_as_float(X1[i] & 0xffff0000u);
    }
#pragma unroll
    for (int i = 0; i < 8; ++i) sum += xs[i] + xs[8 + i];
    sum = xrow_sum(sum16(sum));
    const float mean = sum * (1.0f / DM);
    float sq = 0.f, sq1 = 0.f;
#pragma unroll
    for (int i = 0; i < 8; ++i) { const float d = xs[i] - mean, e = xs[8 + i] - mean; sq = fmaf(d, d, sq); sq1 = fmaf(e, e, sq1); }
    sq += l < 16 ? sq1 : 0.f;
    sq = xrow_sum(sum16(sq));
    const float sd = rbf(sqrtf(rbf(rbf(sq * (1.0f / DM)) + 1e-5f)));
    const float mb = rbf(mean);
    f32x4 d = {0.f, 0.f, 0.f, 0.f};
#pragma unroll
    for (int i = 0; i < 4; ++i) {
      const float xn = rbf(bf2f(xr[i]) - mb) / sd;
      d = mfma(mk & cvtpk(xn, xn), A[i], d);
    }
    if (valid) *(LAS f32x4*)(lds + L_QP + (2 * w + (l >> 5)) * QP_STR + 8 * (l & 7) + 4 * ((l >> 4) & 1)) = d;
    lds_signal(flags + 0, l);
    TS(1);
  }

  u64 tag;
  const int catchup = WIN0 + (int)((unsigned long long)CATCH >> (4 * w) & 15);
#pragma unroll
  for (int i = WIN0; i < 12; ++i) {
    if (((i == PUBPOS + 1 || i == PUBPOS + 2) && w == PUBW) || i < catchup) VMWAIT(WIN);
    else PACE(i);
    K[i] = KLOAD(i);
    SB();
    if (i == PUBPOS) {
      tag = ((u64)__builtin_amdgcn_readfirstlane((unsigned)(epv >> 32)) << 32 | (u64)__builtin_amdgcn_readfirstlane((unsigned)epv)) + 1;
      if (w == PUBW && b < 240) {
        lds_wait_ge(flags + 0, 10);
        float acc = 0.f;
#pragma unroll
        for (int u = 0; u < 20; ++u) acc += lds[L_QP + u * QP_STR + l];
        put_gran(ws + G_Q + b * 64 + l, acc, (unsigned)tag, (unsigned)(tag >> 32) & 0xffu);
        TS(2);
      }
      SB();
    }
  }
  const unsigned tlo = (unsigned)tag, thi = (unsigned)(tag >> 32) & 0xffu;

  u64 g0 = 0, g1 = 0;
  unsigned b1v = 0;
  const int qcol = w < 15 ? h * DH + (w - 12) * 64 + l : HQ * DH + h2 * 48 + (l < 48 ? l : 0);
  gu64* qsrc = ws + G_Q + (qcol >> 6) * 128 + (qcol & 63);
  if (w >= 12) {
    g0 = get_gran(qsrc); g1 = get_gran(qsrc + 64);
    b1v = ((const GAS unsigned*)p.b1)[h2 * 24 + (l < 48 ? l >> 1 : 0)];
  }
  SB();

  float sc[4], mwg;
  v4u qf[3], sel[4], WO[3], W2;
  f32x4 acc[3] = {{0.f, 0.f, 0.f, 0.f}, {0.f, 0.f, 0.f, 0.f}, {0.f, 0.f, 0.f, 0.f}};
  unsigned xfin = 0, b2fin = 0;
  const __amdgpu_buffer_rsrc_t ro = mkrsrc(p.wo + (size_t)(h2 * DH + 12 * w) * DM + s2 * 80, 12 * DM * 2);
  const unsigned oo = (l & 15) < 10 ? (unsigned)((l >> 4) * (DM * 2) + (l & 15) * 16) : OOB;
  auto load_wo = [&](int lo, int hi, bool paced) {
#pragma unroll
    for (int u = lo; u < hi; ++u) {
      if (paced) VMWAIT(WIN - 1);
      if (u == 0) {
        const __amdgpu_buffer_rsrc_t r2 = mkrsrc(p.w2 + (size_t)(h2 * 48 + 4 * (w < 12 ? w : 0)) * DM + s2 * 80, w < 12 ? 4 * DM * 2 : 0);
        W2 = __builtin_amdgcn_raw_buffer_load_b128(r2, oo, 0, W_AUX);
      } else {
        WO[u - 1] = __builtin_amdgcn_raw_buffer_load_b128(ro, oo, (u - 1) * 4 * DM * 2, W_AUX);
      }
      SB();
    }
  };
#pragma unroll
  for (int i = 0; i < 12; ++i) {
    if (i >= EI) VMWAIT(WINE - 1);
    else if (i < WIN && w >= 12) VMWAIT(WIN + 2);
    else PACE(12 + i);
    V[i] = VLOAD(i);
    SB();
    if (i == GI && w >= 12) {
      if (__builtin_expect(!__all(gran_ok(g0, tlo, thi) & gran_ok(g1, tlo, thi)), 0)) {
        for (unsigned spins = 0; spins < SPIN_CAP; ++spins) {
          __builtin_amdgcn_s_sleep(4);
          g0 = get_gran(qsrc); g1 = get_gran(qsrc + 64);
          if (__all(gran_ok(g0, tlo, thi) & gran_ok(g1, tlo, thi))) break;
        }
      }
      const float tot = gran_val(g0) + gran_val(g1);
      if (w < 15) {
        ((LAS unsigned short*)(lds + L_QB))[(w - 12) * 64 + l] = (unsigned short)(cvtpk(tot, tot) & 0xffffu);
      } else {
        float hv = rbf(rbf(tot) + ((l & 1) ? __uint_as_float(b1v & 0xffff0000u) : bf2f(b1v & 0xffffu)));
        hv = hv > 0.f ? hv : 0.f;
        hv = hv * hv;
        if (l < 48) ((LAS unsigned*)(lds + L_HB))[l] = cvtpk(hv, hv);
      }
      lds_signal(flags + 1, l);
      TS(3);
    }
    if (i == QI) {
      TS(4);
      lds_wait_ge(flags + 1, 4);
#pragma unroll
      for (int j = 0; j < 3; ++j) qf[j] = *(LAS v4u*)(lds + L_QB + 32 * j + 4 * (l & 7));
      TS(5);
      sc[0] = qk_score(K[0], K[1], K[2], qf);
      sc[1] = qk_score(K[3], K[4], K[5], qf);
    }
    if (i == QI + 1) {
      sc[2] = qk_score(K[6], K[7], K[8], qf);
      sc[3] = qk_score(K[9], K[10], K[11], qf);
      float mw = fmaxf(fmaxf(sc[0], sc[1]), fmaxf(sc[2], sc[3]));
      mw = xrow_max(fmaxf(mw, dpp<ROR8>(mw)));
      if (l == 0) lds[L_M + w] = mw;
      lds_signal(flags + 2, l);
    }
    if (i == QI + 3) {
      lds_wait_ge(flags + 2, 16);
      mwg = max16(lds[L_M + (l & 15)]);
      float lw = 0.f;
#pragma unroll
      for (int g = 0; g < 4; ++g) {
        const float pr = __builtin_amdgcn_exp2f((sc[g] - mwg) * LOG2E);
        lw += pr;
        sel[g] = mk & cvtpk(pr, pr);
      }
      lw = xrow_sum(lw + dpp<ROR8>(lw));
      if (l == 0) lds[L_L + w] = lw;
      if (MLEARLY) lds_signal(flags + 4, l);
#pragma unroll
      for (int u = 0; u < 6; ++u) acc[u % 3] = mfma(sel[u / 3], V[u], acc[u % 3]);
      TS(6);
    }
    if (i == 10) {
#pragma unroll
      for (int u = 6; u < 9; ++u) acc[u % 3] = mfma(sel[2], V[u], acc[u % 3]);
    }
    SB();
    if (i == WOPOS) load_wo(0, WOSPLIT, true);
    if (NEARLY < 16 && i == WOPOS && (w >= 14 || (DIAGACK && w == 0))) load_wo(WOSPLIT, 4, false);
    if (MLEARLY && i == QI + 5 && w == 11) {
      lds_wait_ge(flags + 4, 16);
      const float lsum = sum16(lds[L_L + (l & 15)]);
      if (l < 16) put_gran(ws + G_O + (size_t)(h * 8 + s) * 128 + 96 + l, l == 0 ? mwg : lsum, tlo, thi);
      SB();
    }
  }
  if (WOSPLIT < 4) lds_signal(flags + 3, l);
  if (WOIMM) {
    lds_wait_ge(flags + 3, WOIMM);
    load_wo(WOSPLIT, 4, false);
  }
#pragma unroll
  for (int u = 9; u < 12; ++u) acc[u % 3] = mfma(sel[3], V[u], acc[u % 3]);
  {
    LAS float* op = lds + L_OP + (w + 16 * (l >> 5)) * OP_STR + 8 * (l & 7) + 4 * ((l >> 4) & 1);
#pragma unroll
    for (int j = 0; j < 3; ++j) if (valid) *(LAS f32x4*)(op + 64 * j) = acc[j];
  }
  TS(7);
  bool late = false;
  if (NEARLY < 16) {
    if (w < 14 && !(DIAGACK && w == 0)) {
      unsigned arr = 0;
      if (l == 0) arr = __hip_atomic_fetch_add(flags + 6 + opaque0(), 1u, RLX_WG);
      late = __builtin_amdgcn_readfirstlane(arr) >= NEARLY;
      if (!late) {
        lds_wait_ge(flags + 3, WOGATE);
        load_wo(WOSPLIT, 4, false);
      }
    }
  } else if (WOSPLIT < 4 && !WOIMM) {
    lds_wait_ge(flags + 3, WOGATE);
    load_wo(WOSPLIT, 4, false);
  }
  TS(15);
  BAR();
  gu64* og = ws + G_O + (size_t)(h2 * 8) * 128;
  gu64* op_ = ws + G_O + (size_t)(h * 8 + s) * 128;
  if (w < 6) {
    const int c4 = tid >> 3, pg = tid & 7;
    LAS float* src = lds + L_OP + pg * OP_STR + 4 * c4;
    f32x4 t = (*(LAS f32x4*)(src) + *(LAS f32x4*)(src + 8 * OP_STR)) + (*(LAS f32x4*)(src + 16 * OP_STR) + *(LAS f32x4*)(src + 24 * OP_STR));
#pragma unroll
    for (int r = 0; r < 4; ++r) t[r] = sum8(t[r]);
    const unsigned pk = (pg & 1) ? cvtpk(t[2], t[3]) : cvtpk(t[0], t[1]);
    if (pg < 2) put_gran2(op_ + 2 * c4 + pg, pk, tlo);
  } else if (!MLEARLY && w == 6) {
    const float lsum = sum16(lds[L_L + (l & 15)]);
    if (l < 16) put_gran(op_ + 96 + l, l == 0 ? mwg : lsum, tlo, thi);
  }
  TS(8);
  if (DIAGACK && w == 0) { VMWAIT(0); TS(1); }
  if (late) {
    if (w >= 6 && LATESLEEP) __builtin_amdgcn_s_sleep(LATESLEEP);
    load_wo(WOSPLIT, 4, false);
  }
  if (w == 10 && h2 < 20) {
    xfin = ((const GAS unsigned*)p.x)[s2 * 40 + h2 * 2 + (l & 1)];
    b2fin = ((const GAS unsigned*)p.b2)[s2 * 40 + h2 * 2 + (l & 1)];
  }
  if (w >= 14) {
    const int d = (w - 14) * 48 + (l < 48 ? l : 0);
    u64 g[8], gm;
    unsigned miss = 0x1ffu;
    float f = 0.f;
    if (GDELAY) __builtin_amdgcn_s_sleep(GDELAY);
    for (unsigned spins = 0;; ++spins) {
#pragma unroll
      for (int i = 0; i < 8; ++i) if (miss >> i & 1u) g[i] = get_gran(og + i * 128 + d);
      unsigned m2 = 0;
      if (miss >> 8) {
        gm = get_gran(og + ((l >> 1) & 7) * 128 + 96 + (l & 1));
        if (__all(gran_ok(gm, tlo, thi))) {
          const float ml = gran_val(gm);
          const float mo = dpp<XOR1>(ml);
          const float ms = (l & 1) ? mo : ml, ls = (l & 1) ? ml : mo;
          const float M = max16(ms);
          const float e = __builtin_amdgcn_exp2f((ms - M) * LOG2E);
          f = e * __builtin_amdgcn_rcpf(0.5f * sum16(e * ls));
        } else {
          m2 = 0x100u;
        }
      }
#pragma unroll
      for (int i = 0; i < 8; ++i) m2 |= __all((unsigned)g[i] == tlo) ? 0u : 1u << i;
      miss = m2;
      if (miss == 0 || spins > SPIN_CAP) break;
      if (!GSKIP) miss |= 0xffu;
      if (GSLEEP) __builtin_amdgcn_s_sleep(GSLEEP);
    }
    float o0 = 0.f, o1 = 0.f;
#pragma unroll
    for (int i = 0; i < 8; ++i) {
      const float fi = __builtin_bit_cast(float, __builtin_amdgcn_readlane(__builtin_bit_cast(int, f), 2 * i));
      const unsigned v = (unsigned)(g[i] >> 32);
      o0 = fmaf(fi, bf2f(v & 0xffffu), o0);
      o1 = fmaf(fi, __uint_as_float(v & 0xffff0000u), o1);
    }
    if (l < 48) ((LAS unsigned*)(lds + L_OB))[d] = cvtpk(o0, o1);
  }
  const unsigned lm = (l & 8) ? 0u : 0xffffffffu;
  LAS float* yp = lds + L_YP + w * YP_STR + 8 * (l & 15) + 4 * (l >> 4);
  const bool yl = (l & 15) < 10 && l < 32;
  const int yc = (w & 1) * 64 + l;
  float zs = 0.f;
  if (w < 12) {
    const unsigned hd = ((LAS unsigned*)(lds + L_HB))[4 * w + (l >> 4)];
    const f32x4 z = mfma(mk & (hd & lm), W2, f32x4{0.f, 0.f, 0.f, 0.f});
    if (yl) *(LAS f32x4*)(yp + 80) = z;
    lds_signal(flags + 5, l);
    if (w < 2) {
      lds_wait_ge(flags + 5, 12);
      if (yc < 80) {
#pragma unroll
        for (int u = 0; u < 12; ++u) zs += lds[L_YP + u * YP_STR + 80 + yc];
      }
      asm volatile("" : "+v"(zs));
    }
  }
  if (DIAGACK && w > 0 && w < 14) { VMWAIT(0); TS(2); }
  TS(9);
  BAR();
  {
    f32x4 y[3];
#pragma unroll
    for (int i = 0; i < 3; ++i) {
      const unsigned ov = ((LAS unsigned short*)(lds + L_OB))[12 * w + 4 * i + (l >> 4)];
      y[i] = mfma(mk & ((ov | (ov << 16)) & lm), WO[i], f32x4{0.f, 0.f, 0.f, 0.f});
    }
    if (yl) *(LAS f32x4*)(yp) = (y[0] + y[1]) + y[2];
  }
  BAR();
  if (w < 2) {
    if (yc < 80) {
      float t[16];
#pragma unroll
      for (int u = 0; u < 16; ++u) t[u] = lds[L_YP + u * YP_STR + yc];
#pragma unroll
      for (int st = 8; st > 0; st >>= 1)
#pragma unroll
        for (int u = 0; u < st; ++u) t[u] += t[u + st];
      put_gran2(ws + G_Y + (size_t)(s2 * 32 + h2) * 80 + yc, cvtpk(t[0], zs), tlo);
    }
    TS(10);
    if (DIAGACK) { VMWAIT(0); TS(3); }
  } else if (w == 10 && h2 < 20) {
    gu64* ys = ws + G_Y + (size_t)(s2 * 32 + (l >> 1)) * 80 + 4 * h2 + 2 * (l & 1);
    u64 y0 = 0, y1 = 0;
    bool ok = false;
    if (FDELAY) __builtin_amdgcn_s_sleep(FDELAY);
    for (unsigned spins = 0;; ++spins) {
      if (!ok || !FSKIP) { y0 = get_gran(ys); y1 = get_gran(ys + 1); }
      ok = ((unsigned)y0 == tlo) & ((unsigned)y1 == tlo);
      if (__all(ok) || spins > SPIN_CAP) break;
      if (FSLEEP) __builtin_amdgcn_s_sleep(FSLEEP);
    }
    const unsigned p0 = (unsigned)(y0 >> 32), p1 = (unsigned)(y1 >> 32);
    float v4[4] = {bf2f(p0 & 0xffffu), bf2f(p1 & 0xffffu), __uint_as_float(p0 & 0xffff0000u), __uint_as_float(p1 & 0xffff0000u)};
#pragma unroll
    for (int r = 0; r < 4; ++r) {
      float t = v4[r];
      t += dpp<XOR2>(t);
      const float u = dpp<HMIR>(t);
      t += dpp<XOR1>(u);
      t += dpp<ROR8>(t);
      v4[r] = xrow_sum(t);
    }
    float res[2];
#pragma unroll
    for (int r = 0; r < 2; ++r) {
      const float sv = rbf(v4[r]);
      const float bb = r == 0 ? bf2f(b2fin & 0xffffu) : __uint_as_float(b2fin & 0xffff0000u);
      const float xx = r == 0 ? bf2f(xfin & 0xffffu) : __uint_as_float(xfin & 0xffff0000u);
      const float o2 = rbf(rbf(v4[2 + r]) + bb);
      const float gt = rbf(o2 * __builtin_amdgcn_rcpf(1.0f + __builtin_amdgcn_exp2f(-o2 * LOG2E)));
      res[r] = xx + rbf(sv * gt);
    }
    if (l < 2) ((GAS unsigned*)p.out)[s2 * 40 + h2 * 2 + l] = cvtpk(res[0], res[1]);
    if (b == 0 && l == 0) __hip_atomic_store(ws + G_EPOCH, tag, RLX_AGENT);
    TS(11);
  }
}

static inline size_t ws_offset(size_t) { return 0; }

extern "C" void kernel_launch(void* const* d_in, const int* in_sizes, int n_in,
                              void* d_out, int out_size, void* d_ws, size_t ws_size,
                              hipStream_t stream) {
  Params p;
  p.x = (const unsigned short*)d_in[0];
  p.k = (const unsigned short*)d_in[1];
  p.v = (const unsigned short*)d_in[2];
  p.wq = (const unsigned short*)d_in[3];
  p.wo = (const unsigned short*)d_in[4];
  p.w1 = (const unsigned short*)d_in[6];
  p.b1 = (const unsigned short*)d_in[7];
  p.w2 = (const unsigned short*)d_in[8];
  p.b2 = (const unsigned short*)d_in[9];
  p.out = (unsigned short*)d_out;
  p.ws = (u64*)((char*)d_ws + ws_offset(ws_size));
  hipLaunchKernelGGL(fused_decode, dim3(NWG), dim3(NT), 0, stream, p);
}
```

```cpp
#include <hip/hip_runtime.h>
#include <hip/hip_bf16.h>
#include <stdint.h>

typedef unsigned v4u __attribute__((ext_vector_type(4)));
typedef unsigned v2u __attribute__((ext_vector_type(2)));
typedef float f32x4 __attribute__((ext_vector_type(4)));
typedef short bf16x8 __attribute__((ext_vector_type(8)));
typedef __bf16 bf16x2_t __attribute__((ext_vector_type(2)));
typedef unsigned long long u64;
#define GAS __attribute__((address_space(1)))
#define LAS __attribute__((address_space(3)))
#define RLX_AGENT __ATOMIC_RELAXED, __HIP_MEMORY_SCOPE_AGENT
#define RLX_WG __ATOMIC_RELAXED, __HIP_MEMORY_SCOPE_WORKGROUP
typedef GAS u64 gu64;

#define WIN 2
#define WIN0 2
#define QI 4
#define GI 1
#define EI 9
#define WINE 4
#define PUBPOS 5
#define WOPOS 8
#define WOSPLIT 1
#define WOIMM 0
#define GDELAY 0
#define FB0 16
#define GSLEEP 0
#define FSLEEP 0
#define FDELAY 0
#define F128 0
#define MLPRE 1
#define DIAGACK 0
#define K_AUX 2
#define V_AUX 2
#define W_AUX 2

constexpr int DM = 640, HQ = 32, DH = 192, SK = 32768, WN = 4096, FF = 1536;
constexpr int NWG = 256, NT = 1024;
constexpr int ROWB = DH * 2;
constexpr int GRPB = 8 * ROWB;
constexpr float LOG2E = 1.4426950408889634f;
constexpr int RSRC_W3 = 0x00020000;
constexpr unsigned SPIN_CAP = 1u << 18;

constexpr size_t G_EPOCH = 0;
constexpr size_t G_Q = 64;
constexpr size_t G_O = G_Q + 256 * 64;
constexpr size_t G_Y = G_O + (size_t)NWG * 128;
constexpr size_t G_END = G_Y + (size_t)240 * 64;
constexpr size_t G_TS = G_END + 64;

constexpr int QP_STR = 68;
constexpr int OP_STR = 200;
constexpr int YP_STR = 80;
constexpr int L_QP = 0;
constexpr int L_OP = L_QP + 20 * QP_STR;
constexpr int L_YP = L_OP + 32 * OP_STR;
constexpr int L_ZP = L_YP + 32 * YP_STR;
constexpr int L_X = L_ZP + 16 * YP_STR;
constexpr int L_QB = L_X + 512;
constexpr int L_OB = L_QB + 96;
constexpr int L_HB = L_OB + 256;
constexpr int L_M = L_HB + 64;
constexpr int L_L = L_M + 16;
constexpr int L_FLAG = L_L + 16;
constexpr int L_END = L_FLAG + 8;

struct Params {
  const unsigned short* x;
  const unsigned short* k;
  const unsigned short* v;
  const unsigned short* wq;
  const unsigned short* wo;
  const unsigned short* w1;
  const unsigned short* b1;
  const unsigned short* w2;
  const unsigned short* b2;
  unsigned short* out;
  u64* ws;
};

__device__ __forceinline__ float bf2f(unsigned h) { return __builtin_bit_cast(float, h << 16); }
__device__ __forceinline__ unsigned cvtpk(float lo, float hi) {
  bf16x2_t r = {(__bf16)lo, (__bf16)hi};
  return __builtin_bit_cast(unsigned, r);
}
__device__ __forceinline__ float rbf(float f) { return bf2f(cvtpk(f, f) & 0xffffu); }
__device__ __forceinline__ float dot2(unsigned a, unsigned b, float acc) {
  return __builtin_amdgcn_fdot2_f32_bf16(__builtin_bit_cast(bf16x2_t, a), __builtin_bit_cast(bf16x2_t, b), acc, false);
}
template <int CTRL> __device__ __forceinline__ float dpp(float x) {
  return __builtin_bit_cast(float, __builtin_amdgcn_mov_dpp(__builtin_bit_cast(int, x), CTRL, 0xf, 0xf, true));
}
constexpr int XOR1 = 0xB1, XOR2 = 0x4E, HMIR = 0x141, ROR8 = 0x128;
__device__ __forceinline__ float sum8(float x) { x += dpp<XOR1>(x); x += dpp<XOR2>(x); x += dpp<HMIR>(x); return x; }
__device__ __forceinline__ float sum16(float x) { x = sum8(x); x += dpp<ROR8>(x); return x; }
__device__ __forceinline__ float max16(float x) {
  x = fmaxf(x, dpp<XOR1>(x)); x = fmaxf(x, dpp<XOR2>(x)); x = fmaxf(x, dpp<HMIR>(x)); x = fmaxf(x, dpp<ROR8>(x)); return x; }
__device__ __forceinline__ float xrow_sum(float x) {
  auto s = __builtin_amdgcn_permlane16_swap(__float_as_uint(x), __float_as_uint(x), false, false);
  x = __uint_as_float(s[0]) + __uint_as_float(s[1]);
  auto t = __builtin_amdgcn_permlane32_swap(__float_as_uint(x), __float_as_uint(x), false, false);
  return __uint_as_float(t[0]) + __uint_as_float(t[1]);
}
__device__ __forceinline__ float xrow_max(float x) {
  auto s = __builtin_amdgcn_permlane16_swap(__float_as_uint(x), __float_as_uint(x), false, false);
  x = fmaxf(__uint_as_float(s[0]), __uint_as_float(s[1]));
  auto t = __builtin_amdgcn_permlane32_swap(__float_as_uint(x), __float_as_uint(x), false, false);
  return fmaxf(__uint_as_float(t[0]), __uint_as_float(t[1]));
}
__device__ __forceinline__ f32x4 mfma(v4u a, v4u b, f32x4 c) {
  return __builtin_amdgcn_mfma_f32_16x16x32_bf16(__builtin_bit_cast(bf16x8, a), __builtin_bit_cast(bf16x8, b), c, 0, 0, 0);
}
__device__ __forceinline__ __amdgpu_buffer_rsrc_t mkrsrc(const void* p, unsigned bytes) {
  return __builtin_amdgcn_make_buffer_rsrc((void*)p, 0, bytes, RSRC_W3);
}
#define SB() __builtin_amdgcn_sched_barrier(0)
#define VMWAIT(n) asm volatile("s_waitcnt vmcnt(%0)" ::"n"(n) : "memory")
#define LGKM0() asm volatile("s_waitcnt lgkmcnt(0)" ::: "memory")
#define BAR() do { LGKM0(); __builtin_amdgcn_s_barrier(); asm volatile("" ::: "memory"); } while (0)

__device__ __forceinline__ void put_gran(gu64* g, float v, unsigned tlo, unsigned thi) {
  const unsigned hi = (__float_as_uint(v) & 0xffffff00u) | thi;
  __hip_atomic_store(g, ((u64)hi << 32) | tlo, RLX_AGENT);
}
__device__ __forceinline__ bool gran_ok(u64 g, unsigned tlo, unsigned thi) {
  return ((unsigned)g == tlo) & (((unsigned)(g >> 32) & 0xffu) == thi);
}
__device__ __forceinline__ float gran_val(u64 g) { return __uint_as_float((unsigned)(g >> 32) & 0xffffff00u); }
__device__ __forceinline__ void put_gran2(gu64* g, unsigned v, unsigned tlo) { __hip_atomic_store(g, ((u64)v << 32) | tlo, RLX_AGENT); }
__device__ __forceinline__ u64 get_gran(gu64* g) { return __hip_atomic_load(g, RLX_AGENT); }

__device__ __forceinline__ void lds_wait_ge(LAS unsigned* f, unsigned n) {
  while (__hip_atomic_load(f, RLX_WG) < n) __builtin_amdgcn_s_sleep(1);
  asm volatile("" ::: "memory");
}
__device__ __forceinline__ int opaque0() { int z; asm("v_mov_b32 %0, 0" : "=v"(z)); return z; }
__device__ __forceinline__ void lds_signal(LAS unsigned* f, int lane) {
  LGKM0();
  if (lane == 0) (void)__hip_atomic_fetch_add(f + opaque0(), 1u, RLX_WG);
}
__device__ __forceinline__ float qk_score(v4u k0, v4u k1, v4u k2, const v4u (&qf)[3]) {
  float d0 = dot2(k0.x, qf[0].x, 0.f), d1 = dot2(k0.y, qf[0].y, 0.f);
  d0 = dot2(k0.z, qf[0].z, d0); d1 = dot2(k0.w, qf[0].w, d1);
  d0 = dot2(k1.x, qf[1].x, d0); d1 = dot2(k1.y, qf[1].y, d1);
  d0 = dot2(k1.z, qf[1].z, d0); d1 = dot2(k1.w, qf[1].w, d1);
  d0 = dot2(k2.x, qf[2].x, d0); d1 = dot2(k2.y, qf[2].y, d1);
  d0 = dot2(k2.z, qf[2].z, d0); d1 = dot2(k2.w, qf[2].w, d1);
  return rbf(rbf(sum8(d0 + d1)) * 0.07216878364870322f);
}
#define KLOAD(i) __builtin_amdgcn_raw_buffer_load_b128(rk, voff + ((i) % 3) * 128, woff + ((i) / 3) * GRPB, K_AUX)
#define VLOAD(i) __builtin_amdgcn_raw_buffer_load_b128(rv, voff + ((i) % 3) * 128, woff + ((i) / 3) * GRPB, V_AUX)

#define TS(i) do {} while (0)

__global__ void __launch_bounds__(NT) fused_decode(Params p) {
  __shared__ __attribute__((aligned(16))) float lds_[L_END];
  LAS float* lds = (LAS float*)lds_;
  LAS unsigned* flags = (LAS unsigned*)(lds + L_FLAG);
  const int tid = threadIdx.x, l = tid & 63, w = __builtin_amdgcn_readfirstlane(tid >> 6);
  const int b = blockIdx.x;
  const int s = b & 7, h = b >> 3;
  const bool outp = b >= 16;
  const int rb = outp ? (b - 16) / 10 : 0, cb = outp ? (b - 16) % 10 : 0;
  TS(0);
  gu64* ws = (gu64*)p.ws;

  const unsigned voff = (unsigned)((l >> 3) * ROWB + (l & 7) * 16);
  const size_t kvoff = ((size_t)h * SK + (SK - WN) + (size_t)s * (WN / 8)) * DH;
  const __amdgpu_buffer_rsrc_t rk = mkrsrc(p.k + kvoff, (WN / 8) * ROWB);
  const __amdgpu_buffer_rsrc_t rv = mkrsrc(p.v + kvoff, (WN / 8) * ROWB);
  const int woff = w * (4 * GRPB);

  const bool proj = w < 10 && b < 240;
  const int row0 = (b & 1) * 320 + 32 * w;
  v4u K[12], V[12], A[4], X0, X1;
  if (__builtin_expect(proj, 1)) {
    const int ld = b < 192 ? HQ * DH : FF;
    const unsigned short* wb = b < 192 ? p.wq + (b >> 1) * 64 : p.w1 + ((b - 192) >> 1) * 64;
    const __amdgpu_buffer_rsrc_t ra = mkrsrc(wb + (size_t)row0 * ld, 32 * ld * 2);
    const __amdgpu_buffer_rsrc_t rx = mkrsrc(p.x, DM * 2);
    const unsigned aoff = (unsigned)((l >> 3) * ld * 2 + (l & 7) * 16);
    X0 = __builtin_amdgcn_raw_buffer_load_b128(rx, l * 16, 0, 0);
    X1 = __builtin_amdgcn_raw_buffer_load_b128(rx, l * 16 + 1024, 0, 0);
#pragma unroll
    for (int i = 0; i < 4; ++i) A[i] = __builtin_amdgcn_raw_buffer_load_b128(ra, aoff, i * 8 * ld * 2, W_AUX);
  }
  SB();
#pragma unroll
  for (int i = 0; i < WIN0; ++i) K[i] = KLOAD(i);
  SB();
  if (tid < 8) flags[tid] = 0;
  TS(12);
  BAR();
  TS(13);
  const u64 epv = get_gran(ws + G_EPOCH);
  SB();

  v4u mk;
  {
    const unsigned hm = (l & 1) ? 0xffff0000u : 0x0000ffffu;
    const int q4 = (l & 7) >> 1;
    mk.x = q4 == 0 ? hm : 0u; mk.y = q4 == 1 ? hm : 0u; mk.z = q4 == 2 ? hm : 0u; mk.w = q4 == 3 ? hm : 0u;
  }
  const bool valid = ((l >> 3) & 1) == (l >> 5);
  const int dcol = 8 * (l & 7) + 4 * ((l >> 4) & 1);
  const f32x4 zero4 = {0.f, 0.f, 0.f, 0.f};

  if (proj) {
    *(LAS v4u*)(lds + L_X + 4 * l) = X0;
    *(LAS v4u*)(lds + L_X + 256 + 4 * l) = X1;
    unsigned xr[4];
#pragma unroll
    for (int i = 0; i < 4; ++i) xr[i] = ((LAS unsigned short*)(lds + L_X))[row0 + 8 * i + (l >> 3)];
    float xs[16], sum = 0.f;
#pragma unroll
    for (int i = 0; i < 4; ++i) {
      xs[2 * i] = bf2f(X0[i] & 0xffffu); xs[2 * i + 1] = __uint_as_float(X0[i] & 0xffff0000u);
      xs[8 + 2 * i] = bf2f(X1[i] & 0xffffu); xs[8 + 2 * i + 1] = __uint_as_float(X1[i] & 0xffff0000u);
    }
#pragma unroll
    for (int i = 0; i < 8; ++i) sum += xs[i] + xs[8 + i];
    sum = xrow_sum(sum16(sum));
    const float mean = sum * (1.0f / DM);
    float sq = 0.f, sq1 = 0.f;
#pragma unroll
    for (int i = 0; i < 8; ++i) { const float d = xs[i] - mean, e = xs[8 + i] - mean; sq = fmaf(d, d, sq); sq1 = fmaf(e, e, sq1); }
    sq += l < 16 ? sq1 : 0.f;
    sq = xrow_sum(sum16(sq));
    const float sd = rbf(sqrtf(rbf(rbf(sq * (1.0f / DM)) + 1e-5f)));
    const float mb = rbf(mean);
    f32x4 d = zero4;
#pragma unroll
    for (int i = 0; i < 4; ++i) {
      const float xn = rbf(bf2f(xr[i]) - mb) / sd;
      d = mfma(mk & cvtpk(xn, xn), A[i], d);
    }
    if (valid) *(LAS f32x4*)(lds + L_QP + (2 * w + (l >> 5)) * QP_STR + dcol) = d;
    lds_signal(flags + 0, l);
    TS(1);
  }

  u64 tag;
#pragma unroll
  for (int i = WIN0; i < 12; ++i) {
    if ((i == PUBPOS + 1 || i == PUBPOS + 2) && w == 10) VMWAIT(WIN);
    else VMWAIT(WIN - 1);
    K[i] = KLOAD(i);
    SB();
    if (i == PUBPOS) {
      tag = ((u64)__builtin_amdgcn_readfirstlane((unsigned)(epv >> 32)) << 32 | (u64)__builtin_amdgcn_readfirstlane((unsigned)epv)) + 1;
      if (w == 10 && b < 240) {
        lds_wait_ge(flags + 0, 10);
        float acc = 0.f;
#pragma unroll
        for (int u = 0; u < 20; ++u) acc += lds[L_QP + u * QP_STR + l];
        put_gran(ws + G_Q + b * 64 + l, acc, (unsigned)tag, (unsigned)(tag >> 32) & 0xffu);
        TS(2);
      }
      SB();
    }
  }
  const unsigned tlo = (unsigned)tag, thi = (unsigned)(tag >> 32) & 0xffu;

  u64 g0 = 0, g1 = 0;
  unsigned b1v = 0;
  gu64* qsrc = ws + G_Q + (w < 15 ? 3 * h + (w - 12) : 96 + rb) * 128 + l;
  if (w >= 12) {
    g0 = get_gran(qsrc); g1 = get_gran(qsrc + 64);
    b1v = ((const GAS unsigned*)p.b1)[rb * 32 + (l >> 1)];
  }
  SB();

  float sc[4], mwg;
  v4u qf[3], sel[4], WO[2], W2;
  f32x4 acc[3] = {zero4, zero4, zero4};
  const unsigned oo = (unsigned)((l >> 3) * (DM * 2) + (l & 7) * 16);
  const __amdgpu_buffer_rsrc_t ro = mkrsrc(p.wo + (size_t)(256 * rb + 16 * w) * DM + 64 * cb, outp ? 16 * DM * 2 : 0);
  auto load_wo = [&](int lo, int hi, bool paced) {
#pragma unroll
    for (int u = lo; u < hi; ++u) {
      if (paced) VMWAIT(WIN - 1);
      if (u == 0) {
        const __amdgpu_buffer_rsrc_t r2 = mkrsrc(p.w2 + (size_t)(64 * rb + 8 * (w & 7)) * DM + 64 * cb, outp && w < 8 ? 8 * DM * 2 : 0);
        W2 = __builtin_amdgcn_raw_buffer_load_b128(r2, oo, 0, W_AUX);
      } else {
        WO[u - 1] = __builtin_amdgcn_raw_buffer_load_b128(ro, oo, (u - 1) * 8 * DM * 2, W_AUX);
      }
      SB();
    }
  };
  const int ha = (128 * rb) / 96;
  gu64* mlp = ws + G_O + (size_t)((ha + ((l >> 4) & 1)) * 8 + ((l >> 1) & 7)) * 128 + 96 + (l & 1);
  u64 gm = 0;
#pragma unroll
  for (int i = 0; i < 12; ++i) {
    if (i >= EI) VMWAIT(WINE - 1);
    else if (i < WIN && w >= 12) VMWAIT(WIN + 2);
    else VMWAIT(WIN - 1);
    V[i] = VLOAD(i);
    SB();
    if (MLPRE && i == 11 && w >= 14) gm = get_gran(mlp);
    if (i == GI && w >= 12) {
      if (__builtin_expect(!__all(gran_ok(g0, tlo, thi) & gran_ok(g1, tlo, thi)), 0)) {
#pragma clang loop unroll(disable)
        for (unsigned spins = 0; spins < SPIN_CAP; ++spins) {
          __builtin_amdgcn_s_sleep(4);
          g0 = get_gran(qsrc); g1 = get_gran(qsrc + 64);
          if (__all(gran_ok(g0, tlo, thi) & gran_ok(g1, tlo, thi))) break;
        }
      }
      const float tot = gran_val(g0) + gran_val(g1);
      if (w < 15) {
        ((LAS unsigned short*)(lds + L_QB))[(w - 12) * 64 + l] = (unsigned short)(cvtpk(tot, tot) & 0xffffu);
      } else {
        float hv = rbf(rbf(tot) + ((l & 1) ? __uint_as_float(b1v & 0xffff0000u) : bf2f(b1v & 0xffffu)));
        hv = hv > 0.f ? hv : 0.f;
        hv = hv * hv;
        ((LAS unsigned*)(lds + L_HB))[l] = cvtpk(hv, hv);
      }
      lds_signal(flags + 1, l);
      TS(3);
    }
    if (i == QI) {
      TS(4);
      lds_wait_ge(flags + 1, 4);
#pragma unroll
      for (int j = 0; j < 3; ++j) qf[j] = *(LAS v4u*)(lds + L_QB + 32 * j + 4 * (l & 7));
      TS(5);
      sc[0] = qk_score(K[0], K[1], K[2], qf);
      sc[1] = qk_score(K[3], K[4], K[5], qf);
    }
    if (i == QI + 1) {
      sc[2] = qk_score(K[6], K[7], K[8], qf);
      sc[3] = qk_score(K[9], K[10], K[11], qf);
      float mw = fmaxf(fmaxf(sc[0], sc[1]), fmaxf(sc[2], sc[3]));
      mw = xrow_max(fmaxf(mw, dpp<ROR8>(mw)));
      if (l == 0) lds[L_M + w] = mw;
      lds_signal(flags + 2, l);
    }
    if (i == QI + 3) {
      lds_wait_ge(flags + 2, 16);
      mwg = max16(lds[L_M + (l & 15)]);
      float lw = 0.f;
#pragma unroll
      for (int g = 0; g < 4; ++g) {
        const float pr = __builtin_amdgcn_exp2f((sc[g] - mwg) * LOG2E);
        lw += pr;
        sel[g] = mk & cvtpk(pr, pr);
      }
      lw = xrow_sum(lw + dpp<ROR8>(lw));
      if (l == 0) lds[L_L + w] = lw;
      lds_signal(flags + 4, l);
#pragma unroll
      for (int u = 0; u < 6; ++u) acc[u % 3] = mfma(sel[u / 3], V[u], acc[u % 3]);
      TS(6);
    }
    if (i == 10) {
#pragma unroll
      for (int u = 6; u < 9; ++u) acc[u % 3] = mfma(sel[2], V[u], acc[u % 3]);
    }
    SB();
    if (i == WOPOS) load_wo(0, WOSPLIT, true);
    if (i == QI + 5 && w == 11) {
      lds_wait_ge(flags + 4, 16);
      const float lsum = sum16(lds[L_L + (l & 15)]);
      if (l < 16) put_gran(ws + G_O + (size_t)b * 128 + 96 + l, l == 0 ? mwg : lsum, tlo, thi);
      SB();
    }
  }
  lds_signal(flags + 3, l);
  if (WOIMM) {
    lds_wait_ge(flags + 3, WOIMM);
    load_wo(WOSPLIT, 3, false);
  }
#pragma unroll
  for (int u = 9; u < 12; ++u) acc[u % 3] = mfma(sel[3], V[u], acc[u % 3]);
  {
    LAS float* op = lds + L_OP + (w + 16 * (l >> 5)) * OP_STR + dcol;
#pragma unroll
    for (int j = 0; j < 3; ++j) if (valid) *(LAS f32x4*)(op + 64 * j) = acc[j];
  }
  TS(7);
  if (!WOIMM) {
    lds_wait_ge(flags + 3, 16);
    load_wo(WOSPLIT, 3, false);
  }
  TS(15);
  BAR();
  if (w < 6) {
    const int c4 = tid >> 3, pg = tid & 7;
    LAS float* src = lds + L_OP + pg * OP_STR + 4 * c4;
    f32x4 t = (*(LAS f32x4*)(src) + *(LAS f32x4*)(src + 8 * OP_STR)) + (*(LAS f32x4*)(src + 16 * OP_STR) + *(LAS f32x4*)(src + 24 * OP_STR));
#pragma unroll
    for (int r = 0; r < 4; ++r) t[r] = sum8(t[r]);
    const unsigned pk = (pg & 1) ? cvtpk(t[2], t[3]) : cvtpk(t[0], t[1]);
    if (pg < 2) put_gran2(ws + G_O + (size_t)b * 128 + 2 * c4 + pg, pk, tlo);
  }
  TS(8);
  unsigned xfin = 0, b2fin = 0;
  const int fb = b - FB0;
  const bool fin = w == 10 && fb >= 0 && fb < 160;
  if (fin) {
    xfin = ((const GAS unsigned*)p.x)[2 * fb + (l & 1)];
    b2fin = ((const GAS unsigned*)p.b2)[2 * fb + (l & 1)];
  }
  gu64* ys = ws + G_Y + (size_t)(((fb >> 4) & 15) * 24 + (l < 48 ? l >> 1 : 0)) * 64 + 4 * (fb & 15) + 2 * (l & 1);
  asm volatile("" : "+v"(ys));
  const int yc4 = l >> 2, ypg = l & 3;
  unsigned zb = 0;
  if (outp) {
    LAS unsigned* osrc = (LAS unsigned*)(lds + L_OB) + 16 * w + (l >> 3);
    LAS float* ydst = lds + L_YP + (2 * w + (l >> 5)) * YP_STR + dcol;
    asm volatile("" : "+v"(osrc), "+v"(ydst));
    LAS float* ysrc = lds + L_YP + ypg * YP_STR + 4 * yc4;
    gu64* ydg = ws + G_Y + (size_t)(cb * 24 + rb) * 64 + l;
    asm volatile("" : "+v"(ysrc), "+v"(ydg));
    if (w >= 14) {
      const int d = (w - 14) * 64 + l, pr = 128 * rb + d;
      const int hh = pr / 96;
      gu64* og = ws + G_O + (size_t)(hh * 8) * 128 + (pr - hh * 96);
      LAS v2u* odst = (LAS v2u*)(lds + L_OB + 2 * d);
      asm volatile("" : "+v"(odst));
      u64 g[8];
      float fi[8];
      if (!__all(gran_ok(gm, tlo, thi))) {
#pragma clang loop unroll(disable)
        for (unsigned spins = 0; spins < SPIN_CAP; ++spins) {
          gm = get_gran(mlp);
          if (__all(gran_ok(gm, tlo, thi))) break;
          __builtin_amdgcn_s_sleep(2);
        }
      }
      {
        const float ml = gran_val(gm);
        const float mo = dpp<XOR1>(ml);
        const float ms = (l & 1) ? mo : ml, ls = (l & 1) ? ml : mo;
        const float M = max16(ms);
        const float e = __builtin_amdgcn_exp2f((ms - M) * LOG2E);
        const float f = e * __builtin_amdgcn_rcpf(0.5f * sum16(e * ls));
#pragma unroll
        for (int i = 0; i < 8; ++i) {
          const float fa = __builtin_bit_cast(float, __builtin_amdgcn_readlane(__builtin_bit_cast(int, f), 2 * i));
          const float fb = __builtin_bit_cast(float, __builtin_amdgcn_readlane(__builtin_bit_cast(int, f), 16 + 2 * i));
          fi[i] = hh == ha ? fa : fb;
          asm volatile("" : "+v"(fi[i]));
        }
      }
      if (GDELAY) __builtin_amdgcn_s_sleep(GDELAY);
      float o0, o1;
      for (unsigned spins = 0;; ++spins) {
#pragma unroll
        for (int i = 0; i < 8; ++i) g[i] = get_gran(og + i * 128);
        SB();
        unsigned bad = 0;
        o0 = 0.f; o1 = 0.f;
#pragma unroll
        for (int i = 0; i < 8; ++i) {
          const unsigned v = (unsigned)(g[i] >> 32);
          bad |= (unsigned)g[i] ^ tlo;
          o0 = fmaf(fi[i], bf2f(v & 0xffffu), o0);
          o1 = fmaf(fi[i], __uint_as_float(v & 0xffff0000u), o1);
          asm volatile("" : "+v"(o0), "+v"(o1), "+v"(bad));
        }
        if (__all(bad == 0) || spins > SPIN_CAP) break;
        if (GSLEEP) __builtin_amdgcn_s_sleep(GSLEEP);
      }
      *odst = v2u{cvtpk(o0, o0), cvtpk(o1, o1)};
    } else if (w < 8) {
      const f32x4 z = mfma(mk & ((LAS unsigned*)(lds + L_HB))[8 * w + (l >> 3)], W2, zero4);
      if (valid) *(LAS f32x4*)(lds + L_ZP + (2 * w + (l >> 5)) * YP_STR + dcol) = z;
      lds_signal(flags + 5, l);
      if (w == 0) {
        lds_wait_ge(flags + 5, 8);
        LAS float* src = lds + L_ZP + ypg * YP_STR + 4 * yc4;
        f32x4 t = (*(LAS f32x4*)(src) + *(LAS f32x4*)(src + 4 * YP_STR)) + (*(LAS f32x4*)(src + 8 * YP_STR) + *(LAS f32x4*)(src + 12 * YP_STR));
#pragma unroll
        for (int r = 0; r < 4; ++r) { t[r] += dpp<XOR1>(t[r]); t[r] += dpp<XOR2>(t[r]); }
        zb = cvtpk(0.f, (ypg & 2) ? ((ypg & 1) ? t[3] : t[2]) : ((ypg & 1) ? t[1] : t[0]));
        asm volatile("" : "+v"(zb));
      }
    }
    if (DIAGACK && w > 0 && w < 14) { VMWAIT(0); TS(2); }
    TS(9);
    BAR();
    {
      const unsigned o0 = osrc[0], o1 = osrc[8];
      f32x4 y = mfma(mk & o0, WO[0], zero4);
      y = mfma(mk & o1, WO[1], y);
      if (valid) *(LAS f32x4*)ydst = y;
    }
    BAR();
    if (w == 0) {
      f32x4 t[8];
#pragma unroll
      for (int u = 0; u < 8; ++u) t[u] = *(LAS f32x4*)(ysrc + 4 * u * YP_STR);
      f32x4 a = ((t[0] + t[1]) + (t[2] + t[3])) + ((t[4] + t[5]) + (t[6] + t[7]));
#pragma unroll
      for (int r = 0; r < 4; ++r) { a[r] += dpp<XOR1>(a[r]); a[r] += dpp<XOR2>(a[r]); }
      const float ys = (ypg & 2) ? ((ypg & 1) ? a[3] : a[2]) : ((ypg & 1) ? a[1] : a[0]);
      put_gran2(ydg, (cvtpk(ys, ys) & 0xffffu) | zb, tlo);
      TS(10);
      if (DIAGACK) { VMWAIT(0); TS(3); }
    }
  }
  if (fin) {
    u64 y0, y1;
    if (FDELAY) __builtin_amdgcn_s_sleep(FDELAY);
    for (unsigned spins = 0;; ++spins) {
      y0 = get_gran(ys); y1 = get_gran(ys + 1);
      if (__all(((unsigned)y0 == tlo) & ((unsigned)y1 == tlo)) || spins > SPIN_CAP) break;
      if (FSLEEP) __builtin_amdgcn_s_sleep(FSLEEP);
    }
    const unsigned p0 = l < 48 ? (unsigned)(y0 >> 32) : 0u, p1 = l < 48 ? (unsigned)(y1 >> 32) : 0u;
    float v4[4] = {bf2f(p0 & 0xffffu), bf2f(p1 & 0xffffu), __uint_as_float(p0 & 0xffff0000u), __uint_as_float(p1 & 0xffff0000u)};
#pragma unroll
    for (int r = 0; r < 4; ++r) {
      float t = v4[r];
      t += dpp<XOR2>(t);
      const float u = dpp<HMIR>(t);
      t += dpp<XOR1>(u);
      t += dpp<ROR8>(t);
      v4[r] = xrow_sum(t);
    }
    float res[2];
#pragma unroll
    for (int r = 0; r < 2; ++r) {
      const float sv = rbf(v4[r]);
      const float bb = r == 0 ? bf2f(b2fin & 0xffffu) : __uint_as_float(b2fin & 0xffff0000u);
      const float xx = r == 0 ? bf2f(xfin & 0xffffu) : __uint_as_float(xfin & 0xffff0000u);
      const float o2 = rbf(rbf(v4[2 + r]) + bb);
      const float gt = rbf(o2 * __builtin_amdgcn_rcpf(1.0f + __builtin_amdgcn_exp2f(-o2 * LOG2E)));
      res[r] = xx + rbf(sv * gt);
    }
    if (l < 2) ((GAS unsigned*)p.out)[2 * fb + l] = cvtpk(res[0], res[1]);
    if (fb == 0 && l == 0) __hip_atomic_store(ws + G_EPOCH, tag, RLX_AGENT);
    TS(11);
  }
}

static inline size_t ws_offset(size_t) { return 0; }

extern "C" void kernel_launch(void* const* d_in, const int* in_sizes, int n_in,
                              void* d_out, int out_size, void* d_ws, size_t ws_size,
                              hipStream_t stream) {
  Params p;
  p.x = (const unsigned short*)d_in[0];
  p.k = (const unsigned short*)d_in[1];
  p.v = (const unsigned short*)d_in[2];
  p.wq = (const unsigned short*)d_in[3];
  p.wo = (const unsigned short*)d_in[4];
  p.w1 = (const unsigned short*)d_in[6];
  p.b1 = (const unsigned short*)d_in[7];
  p.w2 = (const unsigned short*)d_in[8];
  p.b2 = (const unsigned short*)d_in[9];
  p.out = (unsigned short*)d_out;
  p.ws = (u64*)((char*)d_ws + ws_offset(ws_size));
  hipLaunchKernelGGL(fused_decode, dim3(NWG), dim3(NT), 0, stream, p);
}
```

```cpp
#include <hip/hip_runtime.h>
#include <hip/hip_bf16.h>
#include <stdint.h>

typedef unsigned v4u __attribute__((ext_vector_type(4)));
typedef unsigned v2u __attribute__((ext_vector_type(2)));
typedef float f32x4 __attribute__((ext_vector_type(4)));
typedef short bf16x8 __attribute__((ext_vector_type(8)));
typedef __bf16 bf16x2_t __attribute__((ext_vector_type(2)));
typedef unsigned long long u64;
#define GAS __attribute__((address_space(1)))
#define LAS __attribute__((address_space(3)))
#define RLX_AGENT __ATOMIC_RELAXED, __HIP_MEMORY_SCOPE_AGENT
#define RLX_WG __ATOMIC_RELAXED, __HIP_MEMORY_SCOPE_WORKGROUP
typedef GAS u64 gu64;

#define WIN 2
#define WIN0 2
#define QI 4
#define GI 1
#define EI 9
#define WINE 4
#define PUBPOS 5
#define WOPOS 8
#define WOSPLIT 1
#define WOIMM 0
#define GDELAY 0
#define FB0 16
#define GSLEEP 0
#define FSLEEP 0
#define FDELAY 0
#define F128 0
#define MLPRE 1
#define PACEA 900
#define PACEW 0
#define PACEX 0
#define PACEB 0
#define PT 0
#define WOPACE 0
#define WOT0 600
#define WOT1 1200
#define TLBT 0
#define TLBN 8
#define FW16 1
#define FSTR 5
#define DIAGACK 0
#define K_AUX 2
#define V_AUX 2
#define W_AUX 2

constexpr int DM = 640, HQ = 32, DH = 192, SK = 32768, WN = 4096, FF = 1536;
constexpr int NWG = 256, NT = 1024;
constexpr int ROWB = DH * 2;
constexpr int GRPB = 8 * ROWB;
constexpr float LOG2E = 1.4426950408889634f;
constexpr int RSRC_W3 = 0x00020000;
constexpr unsigned SPIN_CAP = 1u << 18;

constexpr size_t G_EPOCH = 0;
constexpr size_t G_Q = 64;
constexpr size_t G_O = G_Q + 256 * 64;
constexpr size_t G_Y = G_O + (size_t)NWG * 128;
constexpr size_t G_END = G_Y + (size_t)240 * 64;
constexpr size_t G_TS = G_END + 64;

constexpr int QP_STR = 68;
constexpr int OP_STR = 200;
constexpr int YP_STR = 80;
constexpr int L_QP = 0;
constexpr int L_OP = L_QP + 20 * QP_STR;
constexpr int L_YP = L_OP + 32 * OP_STR;
constexpr int L_ZP = L_YP + 32 * YP_STR;
constexpr int L_X = L_ZP + 16 * YP_STR;
constexpr int L_QB = L_X + 512;
constexpr int L_OB = L_QB + 96;
constexpr int L_HB = L_OB + 256;
constexpr int L_M = L_HB + 64;
constexpr int L_L = L_M + 16;
constexpr int L_FLAG = L_L + 16;
constexpr int L_END = L_FLAG + 8;

struct Params {
  const unsigned short* x;
  const unsigned short* k;
  const unsigned short* v;
  const unsigned short* wq;
  const unsigned short* wo;
  const unsigned short* w1;
  const unsigned short* b1;
  const unsigned short* w2;
  const unsigned short* b2;
  unsigned short* out;
  u64* ws;
};

__device__ __forceinline__ float bf2f(unsigned h) { return __builtin_bit_cast(float, h << 16); }
__device__ __forceinline__ unsigned cvtpk(float lo, float hi) {
  bf16x2_t r = {(__bf16)lo, (__bf16)hi};
  return __builtin_bit_cast(unsigned, r);
}
__device__ __forceinline__ float rbf(float f) { return bf2f(cvtpk(f, f) & 0xffffu); }
__device__ __forceinline__ float dot2(unsigned a, unsigned b, float acc) {
  return __builtin_amdgcn_fdot2_f32_bf16(__builtin_bit_cast(bf16x2_t, a), __builtin_bit_cast(bf16x2_t, b), acc, false);
}
template <int CTRL> __device__ __forceinline__ float dpp(float x) {
  return __builtin_bit_cast(float, __builtin_amdgcn_mov_dpp(__builtin_bit_cast(int, x), CTRL, 0xf, 0xf, true));
}
constexpr int XOR1 = 0xB1, XOR2 = 0x4E, HMIR = 0x141, ROR8 = 0x128;
__device__ __forceinline__ float sum8(float x) { x += dpp<XOR1>(x); x += dpp<XOR2>(x); x += dpp<HMIR>(x); return x; }
__device__ __forceinline__ float sum16(float x) { x = sum8(x); x += dpp<ROR8>(x); return x; }
__device__ __forceinline__ float max16(float x) {
  x = fmaxf(x, dpp<XOR1>(x)); x = fmaxf(x, dpp<XOR2>(x)); x = fmaxf(x, dpp<HMIR>(x)); x = fmaxf(x, dpp<ROR8>(x)); return x; }
__device__ __forceinline__ float xrow_sum(float x) {
  auto s = __builtin_amdgcn_permlane16_swap(__float_as_uint(x), __float_as_uint(x), false, false);
  x = __uint_as_float(s[0]) + __uint_as_float(s[1]);
  auto t = __builtin_amdgcn_permlane32_swap(__float_as_uint(x), __float_as_uint(x), false, false);
  return __uint_as_float(t[0]) + __uint_as_float(t[1]);
}
__device__ __forceinline__ float xrow_max(float x) {
  auto s = __builtin_amdgcn_permlane16_swap(__float_as_uint(x), __float_as_uint(x), false, false);
  x = fmaxf(__uint_as_float(s[0]), __uint_as_float(s[1]));
  auto t = __builtin_amdgcn_permlane32_swap(__float_as_uint(x), __float_as_uint(x), false, false);
  return fmaxf(__uint_as_float(t[0]), __uint_as_float(t[1]));
}
__device__ __forceinline__ f32x4 mfma(v4u a, v4u b, f32x4 c) {
  return __builtin_amdgcn_mfma_f32_16x16x32_bf16(__builtin_bit_cast(bf16x8, a), __builtin_bit_cast(bf16x8, b), c, 0, 0, 0);
}
__device__ __forceinline__ __amdgpu_buffer_rsrc_t mkrsrc(const void* p, unsigned bytes) {
  return __builtin_amdgcn_make_buffer_rsrc((void*)p, 0, bytes, RSRC_W3);
}
#define SB() __builtin_amdgcn_sched_barrier(0)
#define VMWAIT(n) asm volatile("s_waitcnt vmcnt(%0)" ::"n"(n) : "memory")
#define LGKM0() asm volatile("s_waitcnt lgkmcnt(0)" ::: "memory")
#define BAR() do { LGKM0(); __builtin_amdgcn_s_barrier(); asm volatile("" ::: "memory"); } while (0)

__device__ __forceinline__ void put_gran(gu64* g, float v, unsigned tlo, unsigned thi) {
  const unsigned hi = (__float_as_uint(v) & 0xffffff00u) | thi;
  __hip_atomic_store(g, ((u64)hi << 32) | tlo, RLX_AGENT);
}
__device__ __forceinline__ bool gran_ok(u64 g, unsigned tlo, unsigned thi) {
  return ((unsigned)g == tlo) & (((unsigned)(g >> 32) & 0xffu) == thi);
}
__device__ __forceinline__ float gran_val(u64 g) { return __uint_as_float((unsigned)(g >> 32) & 0xffffff00u); }
__device__ __forceinline__ void put_gran2(gu64* g, unsigned v, unsigned tlo) { __hip_atomic_store(g, ((u64)v << 32) | tlo, RLX_AGENT); }
__device__ __forceinline__ u64 get_gran(gu64* g) { return __hip_atomic_load(g, RLX_AGENT); }

__device__ __forceinline__ void lds_wait_ge(LAS unsigned* f, unsigned n) {
  while (__hip_atomic_load(f, RLX_WG) < n) __builtin_amdgcn_s_sleep(1);
  asm volatile("" ::: "memory");
}
__device__ __forceinline__ int opaque0() { int z; asm("v_mov_b32 %0, 0" : "=v"(z)); return z; }
__device__ __forceinline__ void lds_signal(LAS unsigned* f, int lane) {
  LGKM0();
  if (lane == 0) (void)__hip_atomic_fetch_add(f + opaque0(), 1u, RLX_WG);
}
__device__ __forceinline__ float qk_score(v4u k0, v4u k1, v4u k2, const v4u (&qf)[3]) {
  float d0 = dot2(k0.x, qf[0].x, 0.f), d1 = dot2(k0.y, qf[0].y, 0.f);
  d0 = dot2(k0.z, qf[0].z, d0); d1 = dot2(k0.w, qf[0].w, d1);
  d0 = dot2(k1.x, qf[1].x, d0); d1 = dot2(k1.y, qf[1].y, d1);
  d0 = dot2(k1.z, qf[1].z, d0); d1 = dot2(k1.w, qf[1].w, d1);
  d0 = dot2(k2.x, qf[2].x, d0); d1 = dot2(k2.y, qf[2].y, d1);
  d0 = dot2(k2.z, qf[2].z, d0); d1 = dot2(k2.w, qf[2].w, d1);
  return rbf(rbf(sum8(d0 + d1)) * 0.07216878364870322f);
}
#define PACE(j) do {} while (0)
#define PACET(t) do {} while (0)
#define KLOAD(i) __builtin_amdgcn_raw_buffer_load_b128(rk, voff + ((i) % 3) * 128, woff + ((i) / 3) * GRPB, K_AUX)
#define VLOAD(i) __builtin_amdgcn_raw_buffer_load_b128(rv, voff + ((i) % 3) * 128, woff + ((i) / 3) * GRPB, V_AUX)

#define TS(i) do {} while (0)

__global__ void __launch_bounds__(NT) fused_decode(Params p) {
  __shared__ __attribute__((aligned(16))) float lds_[L_END];
  LAS float* lds = (LAS float*)lds_;
  LAS unsigned* flags = (LAS unsigned*)(lds + L_FLAG);
  const int tid = threadIdx.x, l = tid & 63, w = __builtin_amdgcn_readfirstlane(tid >> 6);
  const int b = blockIdx.x;
  const int s = b & 7, h = b >> 3;
  const bool outp = b >= 16;
  const int rb = outp ? (b - 16) / 10 : 0, cb = outp ? (b - 16) % 10 : 0;
  TS(0);
  gu64* ws = (gu64*)p.ws;

  const unsigned voff = (unsigned)((l >> 3) * ROWB + (l & 7) * 16);
  const size_t kvoff = ((size_t)h * SK + (SK - WN) + (size_t)s * (WN / 8)) * DH;
  const __amdgpu_buffer_rsrc_t rk = mkrsrc(p.k + kvoff, (WN / 8) * ROWB);
  const __amdgpu_buffer_rsrc_t rv = mkrsrc(p.v + kvoff, (WN / 8) * ROWB);
  const int woff = w * (4 * GRPB);

  const bool proj = w < 10 && b < 240;
  const int row0 = (b & 1) * 320 + 32 * w;
  v4u K[12], V[12], A[4], X0, X1;
  if (__builtin_expect(proj, 1)) {
    const int ld = b < 192 ? HQ * DH : FF;
    const unsigned short* wb = b < 192 ? p.wq + (b >> 1) * 64 : p.w1 + ((b - 192) >> 1) * 64;
    const __amdgpu_buffer_rsrc_t ra = mkrsrc(wb + (size_t)row0 * ld, 32 * ld * 2);
    const __amdgpu_buffer_rsrc_t rx = mkrsrc(p.x, DM * 2);
    const unsigned aoff = (unsigned)((l >> 3) * ld * 2 + (l & 7) * 16);
    X0 = __builtin_amdgcn_raw_buffer_load_b128(rx, l * 16, 0, 0);
    X1 = __builtin_amdgcn_raw_buffer_load_b128(rx, l * 16 + 1024, 0, 0);
#pragma unroll
    for (int i = 0; i < 4; ++i) A[i] = __builtin_amdgcn_raw_buffer_load_b128(ra, aoff, i * 8 * ld * 2, W_AUX);
  }
  SB();
#pragma unroll
  for (int i = 0; i < WIN0; ++i) K[i] = KLOAD(i);
  SB();
  if (tid < 8) flags[tid] = 0;
  TS(12);
  BAR();
  TS(13);
  const u64 epv = get_gran(ws + G_EPOCH);
  SB();

  v4u mk;
  {
    const unsigned hm = (l & 1) ? 0xffff0000u : 0x0000ffffu;
    const int q4 = (l & 7) >> 1;
    mk.x = q4 == 0 ? hm : 0u; mk.y = q4 == 1 ? hm : 0u; mk.z = q4 == 2 ? hm : 0u; mk.w = q4 == 3 ? hm : 0u;
  }
  const bool valid = ((l >> 3) & 1) == (l >> 5);
  const int dcol = 8 * (l & 7) + 4 * ((l >> 4) & 1);
  const f32x4 zero4 = {0.f, 0.f, 0.f, 0.f};

  if (proj) {
    *(LAS v4u*)(lds + L_X + 4 * l) = X0;
    *(LAS v4u*)(lds + L_X + 256 + 4 * l) = X1;
    unsigned xr[4];
#pragma unroll
    for (int i = 0; i < 4; ++i) xr[i] = ((LAS unsigned short*)(lds + L_X))[row0 + 8 * i + (l >> 3)];
    float xs[16], sum = 0.f;
#pragma unroll
    for (int i = 0; i < 4; ++i) {
      xs[2 * i] = bf2f(X0[i] & 0xffffu); xs[2 * i + 1] = __uint_as_float(X0[i] & 0xffff0000u);
      xs[8 + 2 * i] = bf2f(X1[i] & 0xffffu); xs[8 + 2 * i + 1] = __uint_as_float(X1[i] & 0xffff0000u);
    }
#pragma unroll
    for (int i = 0; i < 8; ++i) sum += xs[i] + xs[8 + i];
    sum = xrow_sum(sum16(sum));
    const float mean = sum * (1.0f / DM);
    float sq = 0.f, sq1 = 0.f;
#pragma unroll
    for (int i = 0; i < 8; ++i) { const float d = xs[i] - mean, e = xs[8 + i] - mean; sq = fmaf(d, d, sq); sq1 = fmaf(e, e, sq1); }
    sq += l < 16 ? sq1 : 0.f;
    sq = xrow_sum(sum16(sq));
    const float sd = rbf(sqrtf(rbf(rbf(sq * (1.0f / DM)) + 1e-5f)));
    const float mb = rbf(mean);
    f32x4 d = zero4;
#pragma unroll
    for (int i = 0; i < 4; ++i) {
      const float xn = rbf(bf2f(xr[i]) - mb) / sd;
      d = mfma(mk & cvtpk(xn, xn), A[i], d);
    }
    if (valid) *(LAS f32x4*)(lds + L_QP + (2 * w + (l >> 5)) * QP_STR + dcol) = d;
    lds_signal(flags + 0, l);
    TS(1);
  }

  u64 tag;
#pragma unroll
  for (int i = WIN0; i < 12; ++i) {
    if ((i == PUBPOS + 1 || i == PUBPOS + 2) && w == 10) VMWAIT(WIN);
    else VMWAIT(WIN - 1);
    PACE(i);
    K[i] = KLOAD(i);
    SB();
    if (i == PUBPOS) {
      tag = ((u64)__builtin_amdgcn_readfirstlane((unsigned)(epv >> 32)) << 32 | (u64)__builtin_amdgcn_readfirstlane((unsigned)epv)) + 1;
      if (w == 10 && b < 240) {
        lds_wait_ge(flags + 0, 10);
        float acc = 0.f;
#pragma unroll
        for (int u = 0; u < 20; ++u) acc += lds[L_QP + u * QP_STR + l];
        put_gran(ws + G_Q + b * 64 + l, acc, (unsigned)tag, (unsigned)(tag >> 32) & 0xffu);
        TS(2);
      }
      SB();
    }
  }
  const unsigned tlo = (unsigned)tag, thi = (unsigned)(tag >> 32) & 0xffu;

  u64 g0 = 0, g1 = 0;
  unsigned b1v = 0;
  gu64* qsrc = ws + G_Q + (w < 15 ? 3 * h + (w - 12) : 96 + rb) * 128 + l;
  if (w >= 12) {
    g0 = get_gran(qsrc); g1 = get_gran(qsrc + 64);
    b1v = ((const GAS unsigned*)p.b1)[rb * 32 + (l >> 1)];
  }
  SB();

  float sc[4], mwg;
  v4u qf[3], sel[4], WO[2], W2;
  f32x4 acc[3] = {zero4, zero4, zero4};
  const unsigned oo = (unsigned)((l >> 3) * (DM * 2) + (l & 7) * 16);
  const __amdgpu_buffer_rsrc_t ro = mkrsrc(p.wo + (size_t)(256 * rb + 16 * w) * DM + 64 * cb, outp ? 16 * DM * 2 : 0);
  auto load_wo = [&](int lo, int hi, bool paced) {
#pragma unroll
    for (int u = lo; u < hi; ++u) {
      if (paced) VMWAIT(WIN - 1);
      if (u == 0) {
        const __amdgpu_buffer_rsrc_t r2 = mkrsrc(p.w2 + (size_t)(64 * rb + 8 * (w & 7)) * DM + 64 * cb, outp && w < 8 ? 8 * DM * 2 : 0);
        W2 = __builtin_amdgcn_raw_buffer_load_b128(r2, oo, 0, W_AUX);
      } else {
        WO[u - 1] = __builtin_amdgcn_raw_buffer_load_b128(ro, oo, (u - 1) * 8 * DM * 2, W_AUX);
      }
      SB();
    }
  };
  const int ha = (128 * rb) / 96;
  gu64* mlp = ws + G_O + (size_t)((ha + ((l >> 4) & 1)) * 8 + ((l >> 1) & 7)) * 128 + 96 + (l & 1);
  u64 gm = 0;
  const int d = (w & 1) * 64 + l, pr = 128 * rb + d;
  const int hh = pr / 96;
  gu64* og = ws + G_O + (size_t)(hh * 8) * 128 + (pr - hh * 96);
  const int fb = b - FB0;
  const bool fin = w == 10 && fb >= 0 && fb < 40 * FSTR && fb % FSTR == 0;
  const int fq = fb / FSTR;
  gu64* ys = ws + G_Y + (size_t)(((fq >> 2) & 15) * 24 + (l >> 4)) * 64 + 16 * (fq & 3) + (l & 15);
  unsigned tlbv = 0;
  unsigned ptv[8] = {0, 0, 0, 0, 0, 0, 0, 0};
#pragma unroll
  for (int i = 0; i < 12; ++i) {
    if (PT && (i == PT + 1 || i == PT + 2) && w >= 14) VMWAIT(WIN + 7);
    else if (PT && (i == PT + 1 || i == PT + 2) && w == 10) VMWAIT(WIN);
    else if (i >= EI) VMWAIT(WINE - 1);
    else if (i < WIN && w >= 12) VMWAIT(WIN + 2);
    else VMWAIT(WIN - 1);
    PACE(12 + i);
    V[i] = VLOAD(i);
    SB();
    if (MLPRE && i == 11 && w >= 14) gm = get_gran(mlp);
    if (PT && i == PT) {
      if (w >= 14) {
#pragma unroll
        for (int u = 0; u < 8; ++u) ptv[u] = __hip_atomic_load((GAS unsigned*)(og + u * 128), RLX_AGENT);
      } else if (fin) {
        ptv[0] = __hip_atomic_load((GAS unsigned*)ys, RLX_AGENT);
      }
      SB();
    }
    if (TLBT && i == TLBT && w == 9 && outp) {
      if (l < TLBN) tlbv = __builtin_nontemporal_load((const GAS unsigned*)(p.wo + (size_t)(256 * rb + (256 / TLBN) * l) * DM + 64 * cb));
      SB();
    }
    if (TLBT && i == TLBT + 3) asm volatile("" ::"v"(tlbv));
    if (PT && i == PT + 3) asm volatile("" ::"v"(ptv[0]), "v"(ptv[1]), "v"(ptv[2]), "v"(ptv[3]), "v"(ptv[4]), "v"(ptv[5]), "v"(ptv[6]), "v"(ptv[7]));
    if (i == GI && w >= 12) {
      if (__builtin_expect(!__all(gran_ok(g0, tlo, thi) & gran_ok(g1, tlo, thi)), 0)) {
#pragma clang loop unroll(disable)
        for (unsigned spins = 0; spins < SPIN_CAP; ++spins) {
          __builtin_amdgcn_s_sleep(4);
          g0 = get_gran(qsrc); g1 = get_gran(qsrc + 64);
          if (__all(gran_ok(g0, tlo, thi) & gran_ok(g1, tlo, thi))) break;
        }
      }
      const float tot = gran_val(g0) + gran_val(g1);
      if (w < 15) {
        ((LAS unsigned short*)(lds + L_QB))[(w - 12) * 64 + l] = (unsigned short)(cvtpk(tot, tot) & 0xffffu);
      } else {
        float hv = rbf(rbf(tot) + ((l & 1) ? __uint_as_float(b1v & 0xffff0000u) : bf2f(b1v & 0xffffu)));
        hv = hv > 0.f ? hv : 0.f;
        hv = hv * hv;
        ((LAS unsigned*)(lds + L_HB))[l] = cvtpk(hv, hv);
      }
      lds_signal(flags + 1, l);
      TS(3);
    }
    if (i == QI) {
      TS(4);
      lds_wait_ge(flags + 1, 4);
#pragma unroll
      for (int j = 0; j < 3; ++j) qf[j] = *(LAS v4u*)(lds + L_QB + 32 * j + 4 * (l & 7));
      TS(5);
      sc[0] = qk_score(K[0], K[1], K[2], qf);
      sc[1] = qk_score(K[3], K[4], K[5], qf);
    }
    if (i == QI + 1) {
      sc[2] = qk_score(K[6], K[7], K[8], qf);
      sc[3] = qk_score(K[9], K[10], K[11], qf);
      float mw = fmaxf(fmaxf(sc[0], sc[1]), fmaxf(sc[2], sc[3]));
      mw = xrow_max(fmaxf(mw, dpp<ROR8>(mw)));
      if (l == 0) lds[L_M + w] = mw;
      lds_signal(flags + 2, l);
    }
    if (i == QI + 3) {
      lds_wait_ge(flags + 2, 16);
      mwg = max16(lds[L_M + (l & 15)]);
      float lw = 0.f;
#pragma unroll
      for (int g = 0; g < 4; ++g) {
        const float pr = __builtin_amdgcn_exp2f((sc[g] - mwg) * LOG2E);
        lw += pr;
        sel[g] = mk & cvtpk(pr, pr);
      }
      lw = xrow_sum(lw + dpp<ROR8>(lw));
      if (l == 0) lds[L_L + w] = lw;
      lds_signal(flags + 4, l);
#pragma unroll
      for (int u = 0; u < 6; ++u) acc[u % 3] = mfma(sel[u / 3], V[u], acc[u % 3]);
      TS(6);
    }
    if (i == 10) {
#pragma unroll
      for (int u = 6; u < 9; ++u) acc[u % 3] = mfma(sel[2], V[u], acc[u % 3]);
    }
    SB();
    if (i == WOPOS) load_wo(0, WOSPLIT, true);
    if (i == QI + 5 && w == 11) {
      lds_wait_ge(flags + 4, 16);
      const float lsum = sum16(lds[L_L + (l & 15)]);
      if (l < 16) put_gran(ws + G_O + (size_t)b * 128 + 96 + l, l == 0 ? mwg : lsum, tlo, thi);
      SB();
    }
  }
  if (WOPACE) {
    PACET(PACEA + PACEB * 23 + PACEX + WOT0);
    load_wo(WOSPLIT, WOPACE == 1 ? 2 : 3, false);
  } else {
    lds_signal(flags + 3, l);
    if (WOIMM) {
      lds_wait_ge(flags + 3, WOIMM);
      load_wo(WOSPLIT, 3, false);
    }
  }
#pragma unroll
  for (int u = 9; u < 12; ++u) acc[u % 3] = mfma(sel[3], V[u], acc[u % 3]);
  {
    LAS float* op = lds + L_OP + (w + 16 * (l >> 5)) * OP_STR + dcol;
#pragma unroll
    for (int j = 0; j < 3; ++j) if (valid) *(LAS f32x4*)(op + 64 * j) = acc[j];
  }
  TS(7);
  if (!WOIMM && !WOPACE) {
    lds_wait_ge(flags + 3, 16);
    load_wo(WOSPLIT, 3, false);
  }
  TS(15);
  BAR();
  if (w < 6) {
    const int c4 = tid >> 3, pg = tid & 7;
    LAS float* src = lds + L_OP + pg * OP_STR + 4 * c4;
    f32x4 t = (*(LAS f32x4*)(src) + *(LAS f32x4*)(src + 8 * OP_STR)) + (*(LAS f32x4*)(src + 16 * OP_STR) + *(LAS f32x4*)(src + 24 * OP_STR));
#pragma unroll
    for (int r = 0; r < 4; ++r) t[r] = sum8(t[r]);
    const unsigned pk = (pg & 1) ? cvtpk(t[2], t[3]) : cvtpk(t[0], t[1]);
    if (pg < 2) put_gran2(ws + G_O + (size_t)b * 128 + 2 * c4 + pg, pk, tlo);
  }
  TS(8);
  if (WOPACE == 1) {
    PACET(PACEA + PACEB * 23 + PACEX + WOT1);
    load_wo(2, 3, false);
  }
  unsigned xfin = 0, b2fin = 0;
  if (fin) {
    xfin = ((const GAS unsigned*)p.x)[8 * fq + ((l & 15) >> 1)];
    b2fin = ((const GAS unsigned*)p.b2)[8 * fq + ((l & 15) >> 1)];
  }
  asm volatile("" : "+v"(ys));
  const int yc4 = l >> 2, ypg = l & 3;
  unsigned zb = 0;
  if (outp) {
    LAS unsigned* osrc = (LAS unsigned*)(lds + L_OB) + 16 * w + (l >> 3);
    LAS float* ydst = lds + L_YP + (2 * w + (l >> 5)) * YP_STR + dcol;
    asm volatile("" : "+v"(osrc), "+v"(ydst));
    LAS float* ysrc = lds + L_YP + ypg * YP_STR + 4 * yc4;
    gu64* ydg = ws + G_Y + (size_t)(cb * 24 + rb) * 64 + l;
    asm volatile("" : "+v"(ysrc), "+v"(ydg));
    if (w >= 14) {
      LAS v2u* odst = (LAS v2u*)(lds + L_OB + 2 * d);
      asm volatile("" : "+v"(odst));
      u64 g[8];
      float fi[8];
      if (!__all(gran_ok(gm, tlo, thi))) {
#pragma clang loop unroll(disable)
        for (unsigned spins = 0; spins < SPIN_CAP; ++spins) {
          gm = get_gran(mlp);
          if (__all(gran_ok(gm, tlo, thi))) break;
          __builtin_amdgcn_s_sleep(2);
        }
      }
      {
        const float ml = gran_val(gm);
        const float mo = dpp<XOR1>(ml);
        const float ms = (l & 1) ? mo : ml, ls = (l & 1) ? ml : mo;
        const float M = max16(ms);
        const float e = __builtin_amdgcn_exp2f((ms - M) * LOG2E);
        const float f = e * __builtin_amdgcn_rcpf(0.5f * sum16(e * ls));
#pragma unroll
        for (int i = 0; i < 8; ++i) {
          const float fa = __builtin_bit_cast(float, __builtin_amdgcn_readlane(__builtin_bit_cast(int, f), 2 * i));
          const float fb = __builtin_bit_cast(float, __builtin_amdgcn_readlane(__builtin_bit_cast(int, f), 16 + 2 * i));
          fi[i] = hh == ha ? fa : fb;
          asm volatile("" : "+v"(fi[i]));
        }
      }
      if (GDELAY) __builtin_amdgcn_s_sleep(GDELAY);
      float o0, o1;
      for (unsigned spins = 0;; ++spins) {
#pragma unroll
        for (int i = 0; i < 8; ++i) g[i] = get_gran(og + i * 128);
        SB();
        unsigned bad = 0;
        o0 = 0.f; o1 = 0.f;
#pragma unroll
        for (int i = 0; i < 8; ++i) {
          const unsigned v = (unsigned)(g[i] >> 32);
          bad |= (unsigned)g[i] ^ tlo;
          o0 = fmaf(fi[i], bf2f(v & 0xffffu), o0);
          o1 = fmaf(fi[i], __uint_as_float(v & 0xffff0000u), o1);
          asm volatile("" : "+v"(o0), "+v"(o1), "+v"(bad));
        }
        if (__all(bad == 0) || spins > SPIN_CAP) break;
        if (GSLEEP) __builtin_amdgcn_s_sleep(GSLEEP);
      }
      *odst = v2u{cvtpk(o0, o0), cvtpk(o1, o1)};
    } else if (w < 8) {
      const f32x4 z = mfma(mk & ((LAS unsigned*)(lds + L_HB))[8 * w + (l >> 3)], W2, zero4);
      if (valid) *(LAS f32x4*)(lds + L_ZP + (2 * w + (l >> 5)) * YP_STR + dcol) = z;
      lds_signal(flags + 5, l);
      if (w == 0) {
        lds_wait_ge(flags + 5, 8);
        LAS float* src = lds + L_ZP + ypg * YP_STR + 4 * yc4;
        f32x4 t = (*(LAS f32x4*)(src) + *(LAS f32x4*)(src + 4 * YP_STR)) + (*(LAS f32x4*)(src + 8 * YP_STR) + *(LAS f32x4*)(src + 12 * YP_STR));
#pragma unroll
        for (int r = 0; r < 4; ++r) { t[r] += dpp<XOR1>(t[r]); t[r] += dpp<XOR2>(t[r]); }
        zb = cvtpk(0.f, (ypg & 2) ? ((ypg & 1) ? t[3] : t[2]) : ((ypg & 1) ? t[1] : t[0]));
        asm volatile("" : "+v"(zb));
      }
    }
    if (DIAGACK && w > 0 && w < 14) { VMWAIT(0); TS(2); }
    TS(9);
    BAR();
    {
      const unsigned o0 = osrc[0], o1 = osrc[8];
      f32x4 y = mfma(mk & o0, WO[0], zero4);
      y = mfma(mk & o1, WO[1], y);
      if (valid) *(LAS f32x4*)ydst = y;
    }
    BAR();
    if (w == 0) {
      f32x4 t[8];
#pragma unroll
      for (int u = 0; u < 8; ++u) t[u] = *(LAS f32x4*)(ysrc + 4 * u * YP_STR);
      f32x4 a = ((t[0] + t[1]) + (t[2] + t[3])) + ((t[4] + t[5]) + (t[6] + t[7]));
#pragma unroll
      for (int r = 0; r < 4; ++r) { a[r] += dpp<XOR1>(a[r]); a[r] += dpp<XOR2>(a[r]); }
      const float ys = (ypg & 2) ? ((ypg & 1) ? a[3] : a[2]) : ((ypg & 1) ? a[1] : a[0]);
      put_gran2(ydg, (cvtpk(ys, ys) & 0xffffu) | zb, tlo);
      TS(10);
      if (DIAGACK) { VMWAIT(0); TS(3); }
    }
  }
  if (fin) {
    float sy, sz;
    if (FDELAY) __builtin_amdgcn_s_sleep(FDELAY);
    for (unsigned spins = 0;; ++spins) {
      u64 g[6];
#pragma unroll
      for (int u = 0; u < 6; ++u) g[u] = get_gran(ys + u * 4 * 64);
      SB();
      unsigned bad = 0;
      sy = 0.f; sz = 0.f;
#pragma unroll
      for (int u = 0; u < 6; ++u) {
        const unsigned v = (unsigned)(g[u] >> 32);
        bad |= (unsigned)g[u] ^ tlo;
        sy += bf2f(v & 0xffffu); sz += __uint_as_float(v & 0xffff0000u);
        asm volatile("" : "+v"(sy), "+v"(sz), "+v"(bad));
      }
      if (__all(bad == 0) || spins > SPIN_CAP) break;
      if (FSLEEP) __builtin_amdgcn_s_sleep(FSLEEP);
    }
    sy = xrow_sum(sy); sz = xrow_sum(sz);
    const float bb = (l & 1) ? __uint_as_float(b2fin & 0xffff0000u) : bf2f(b2fin & 0xffffu);
    const float xx = (l & 1) ? __uint_as_float(xfin & 0xffff0000u) : bf2f(xfin & 0xffffu);
    const float o2 = rbf(rbf(sz) + bb);
    const float gt = rbf(o2 * __builtin_amdgcn_rcpf(1.0f + __builtin_amdgcn_exp2f(-o2 * LOG2E)));
    const float res = xx + rbf(rbf(sy) * gt);
    const unsigned pk = cvtpk(res, dpp<XOR1>(res));
    if (l < 16 && !(l & 1)) ((GAS unsigned*)p.out)[8 * fq + (l >> 1)] = pk;
    if (fb == 0 && l == 0) __hip_atomic_store(ws + G_EPOCH, tag, RLX_AGENT);
    TS(11);
  }
}

static inline size_t ws_offset(size_t) { return 0; }

extern "C" void kernel_launch(void* const* d_in, const int* in_sizes, int n_in,
                              void* d_out, int out_size, void* d_ws, size_t ws_size,
                              hipStream_t stream) {
  Params p;
  p.x = (const unsigned short*)d_in[0];
  p.k = (const unsigned short*)d_in[1];
  p.v = (const unsigned short*)d_in[2];
  p.wq = (const unsigned short*)d_in[3];
  p.wo = (const unsigned short*)d_in[4];
  p.w1 = (const unsigned short*)d_in[6];
  p.b1 = (const unsigned short*)d_in[7];
  p.w2 = (const unsigned short*)d_in[8];
  p.b2 = (const unsigned short*)d_in[9];
  p.out = (unsigned short*)d_out;
  p.ws = (u64*)((char*)d_ws + ws_offset(ws_size));
  hipLaunchKernelGGL(fused_decode, dim3(NWG), dim3(NT), 0, stream, p);
}
```

```cpp
#include <hip/hip_runtime.h>
#include <hip/hip_bf16.h>
#include <stdint.h>

typedef unsigned v4u __attribute__((ext_vector_type(4)));
typedef unsigned v2u __attribute__((ext_vector_type(2)));
typedef float f32x4 __attribute__((ext_vector_type(4)));
typedef short bf16x8 __attribute__((ext_vector_type(8)));
typedef __bf16 bf16x2_t __attribute__((ext_vector_type(2)));
typedef unsigned long long u64;
#define GAS __attribute__((address_space(1)))
#define LAS __attribute__((address_space(3)))
#define RLX_AGENT __ATOMIC_RELAXED, __HIP_MEMORY_SCOPE_AGENT
#define RLX_WG __ATOMIC_RELAXED, __HIP_MEMORY_SCOPE_WORKGROUP
typedef GAS u64 gu64;

#define WIN 2
#define WIN0 2
#define QI 4
#define GI 1
#define EI 9
#define WINE 4
#define PUBPOS 5
#define WOPOS 8
#define WOSPLIT 1
#define WOIMM 0
#define GDELAY 0
#define FB0 16
#define GSLEEP 0
#define FSLEEP 0
#define FDELAY 0
#define F128 0
#define MLPRE 1
#define PACEA 900
#define PACEW 0
#define PACEX 0
#define PACEB 620
#define PT 0
#define WOPACE 1
#define WOT0 600
#define WOT1 1200
#define TLBT 0
#define TLBN 8
#define FW16 1
#define FSTR 5
#define DIAGACK 0
#define K_AUX 2
#define V_AUX 2
#define W_AUX 2

constexpr int DM = 640, HQ = 32, DH = 192, SK = 32768, WN = 4096, FF = 1536;
constexpr int NWG = 256, NT = 1024;
constexpr int ROWB = DH * 2;
constexpr int GRPB = 8 * ROWB;
constexpr float LOG2E = 1.4426950408889634f;
constexpr int RSRC_W3 = 0x00020000;
constexpr unsigned SPIN_CAP = 1u << 18;

constexpr size_t G_EPOCH = 0;
constexpr size_t G_Q = 64;
constexpr size_t G_O = G_Q + 256 * 64;
constexpr size_t G_Y = G_O + (size_t)NWG * 128;
constexpr size_t G_END = G_Y + (size_t)240 * 64;
constexpr size_t G_TS = G_END + 64;

constexpr int QP_STR = 68;
constexpr int OP_STR = 200;
constexpr int YP_STR = 80;
constexpr int L_QP = 0;
constexpr int L_OP = L_QP + 20 * QP_STR;
constexpr int L_YP = L_OP + 32 * OP_STR;
constexpr int L_ZP = L_YP + 32 * YP_STR;
constexpr int L_X = L_ZP + 16 * YP_STR;
constexpr int L_QB = L_X + 512;
constexpr int L_OB = L_QB + 96;
constexpr int L_HB = L_OB + 256;
constexpr int L_M = L_HB + 64;
constexpr int L_L = L_M + 16;
constexpr int L_FLAG = L_L + 16;
constexpr int L_END = L_FLAG + 8;

struct Params {
  const unsigned short* x;
  const unsigned short* k;
  const unsigned short* v;
  const unsigned short* wq;
  const unsigned short* wo;
  const unsigned short* w1;
  const unsigned short* b1;
  const unsigned short* w2;
  const unsigned short* b2;
  unsigned short* out;
  u64* ws;
};

__device__ __forceinline__ float bf2f(unsigned h) { return __builtin_bit_cast(float, h << 16); }
__device__ __forceinline__ unsigned cvtpk(float lo, float hi) {
  bf16x2_t r = {(__bf16)lo, (__bf16)hi};
  return __builtin_bit_cast(unsigned, r);
}
__device__ __forceinline__ float rbf(float f) { return bf2f(cvtpk(f, f) & 0xffffu); }
__device__ __forceinline__ float dot2(unsigned a, unsigned b, float acc) {
  return __builtin_amdgcn_fdot2_f32_bf16(__builtin_bit_cast(bf16x2_t, a), __builtin_bit_cast(bf16x2_t, b), acc, false);
}
template <int CTRL> __device__ __forceinline__ float dpp(float x) {
  return __builtin_bit_cast(float, __builtin_amdgcn_mov_dpp(__builtin_bit_cast(int, x), CTRL, 0xf, 0xf, true));
}
constexpr int XOR1 = 0xB1, XOR2 = 0x4E, HMIR = 0x141, ROR8 = 0x128;
__device__ __forceinline__ float sum8(float x) { x += dpp<XOR1>(x); x += dpp<XOR2>(x); x += dpp<HMIR>(x); return x; }
__device__ __forceinline__ float sum16(float x) { x = sum8(x); x += dpp<ROR8>(x); return x; }
__device__ __forceinline__ float max16(float x) {
  x = fmaxf(x, dpp<XOR1>(x)); x = fmaxf(x, dpp<XOR2>(x)); x = fmaxf(x, dpp<HMIR>(x)); x = fmaxf(x, dpp<ROR8>(x)); return x; }
__device__ __forceinline__ float xrow_sum(float x) {
  auto s = __builtin_amdgcn_permlane16_swap(__float_as_uint(x), __float_as_uint(x), false, false);
  x = __uint_as_float(s[0]) + __uint_as_float(s[1]);
  auto t = __builtin_amdgcn_permlane32_swap(__float_as_uint(x), __float_as_uint(x), false, false);
  return __uint_as_float(t[0]) + __uint_as_float(t[1]);
}
__device__ __forceinline__ float xrow_max(float x) {
  auto s = __builtin_amdgcn_permlane16_swap(__float_as_uint(x), __float_as_uint(x), false, false);
  x = fmaxf(__uint_as_float(s[0]), __uint_as_float(s[1]));
  auto t = __builtin_amdgcn_permlane32_swap(__float_as_uint(x), __float_as_uint(x), false, false);
  return fmaxf(__uint_as_float(t[0]), __uint_as_float(t[1]));
}
__device__ __forceinline__ f32x4 mfma(v4u a, v4u b, f32x4 c) {
  return __builtin_amdgcn_mfma_f32_16x16x32_bf16(__builtin_bit_cast(bf16x8, a), __builtin_bit_cast(bf16x8, b), c, 0, 0, 0);
}
__device__ __forceinline__ __amdgpu_buffer_rsrc_t mkrsrc(const void* p, unsigned bytes) {
  return __builtin_amdgcn_make_buffer_rsrc((void*)p, 0, bytes, RSRC_W3);
}
#define SB() __builtin_amdgcn_sched_barrier(0)
#define VMWAIT(n) asm volatile("s_waitcnt vmcnt(%0)" ::"n"(n) : "memory")
#define LGKM0() asm volatile("s_waitcnt lgkmcnt(0)" ::: "memory")
#define BAR() do { LGKM0(); __builtin_amdgcn_s_barrier(); asm volatile("" ::: "memory"); } while (0)

__device__ __forceinline__ void put_gran(gu64* g, float v, unsigned tlo, unsigned thi) {
  const unsigned hi = (__float_as_uint(v) & 0xffffff00u) | thi;
  __hip_atomic_store(g, ((u64)hi << 32) | tlo, RLX_AGENT);
}
__device__ __forceinline__ bool gran_ok(u64 g, unsigned tlo, unsigned thi) {
  return ((unsigned)g == tlo) & (((unsigned)(g >> 32) & 0xffu) == thi);
}
__device__ __forceinline__ float gran_val(u64 g) { return __uint_as_float((unsigned)(g >> 32) & 0xffffff00u); }
__device__ __forceinline__ void put_gran2(gu64* g, unsigned v, unsigned tlo) { __hip_atomic_store(g, ((u64)v << 32) | tlo, RLX_AGENT); }
__device__ __forceinline__ u64 get_gran(gu64* g) { return __hip_atomic_load(g, RLX_AGENT); }

__device__ __forceinline__ void lds_wait_ge(LAS unsigned* f, unsigned n) {
  while (__hip_atomic_load(f, RLX_WG) < n) __builtin_amdgcn_s_sleep(1);
  asm volatile("" ::: "memory");
}
__device__ __forceinline__ int opaque0() { int z; asm("v_mov_b32 %0, 0" : "=v"(z)); return z; }
__device__ __forceinline__ void lds_signal(LAS unsigned* f, int lane) {
  LGKM0();
  if (lane == 0) (void)__hip_atomic_fetch_add(f + opaque0(), 1u, RLX_WG);
}
__device__ __forceinline__ float qk_score(v4u k0, v4u k1, v4u k2, const v4u (&qf)[3]) {
  float d0 = dot2(k0.x, qf[0].x, 0.f), d1 = dot2(k0.y, qf[0].y, 0.f);
  d0 = dot2(k0.z, qf[0].z, d0); d1 = dot2(k0.w, qf[0].w, d1);
  d0 = dot2(k1.x, qf[1].x, d0); d1 = dot2(k1.y, qf[1].y, d1);
  d0 = dot2(k1.z, qf[1].z, d0); d1 = dot2(k1.w, qf[1].w, d1);
  d0 = dot2(k2.x, qf[2].x, d0); d1 = dot2(k2.y, qf[2].y, d1);
  d0 = dot2(k2.z, qf[2].z, d0); d1 = dot2(k2.w, qf[2].w, d1);
  return rbf(rbf(sum8(d0 + d1)) * 0.07216878364870322f);
}
#define PACET(t) do { while ((int)((unsigned)__builtin_amdgcn_s_memrealtime() - tref) < (t) / 10) __builtin_amdgcn_s_sleep(1); } while (0)
#define PACE(j) PACET(PACEA + PACEB * (j) + ((j) > 12 + WOPOS ? PACEX : 0))
#define KLOAD(i) __builtin_amdgcn_raw_buffer_load_b128(rk, voff + ((i) % 3) * 128, woff + ((i) / 3) * GRPB, K_AUX)
#define VLOAD(i) __builtin_amdgcn_raw_buffer_load_b128(rv, voff + ((i) % 3) * 128, woff + ((i) / 3) * GRPB, V_AUX)

#define TS(i) do {} while (0)

__global__ void __launch_bounds__(NT) fused_decode(Params p) {
  __shared__ __attribute__((aligned(16))) float lds_[L_END];
  LAS float* lds = (LAS float*)lds_;
  LAS unsigned* flags = (LAS unsigned*)(lds + L_FLAG);
  const int tid = threadIdx.x, l = tid & 63, w = __builtin_amdgcn_readfirstlane(tid >> 6);
  const int b = blockIdx.x;
  const int s = b & 7, h = b >> 3;
  const bool outp = b >= 16;
  const int rb = outp ? (b - 16) / 10 : 0, cb = outp ? (b - 16) % 10 : 0;
  TS(0);
  gu64* ws = (gu64*)p.ws;
  const unsigned tref = (unsigned)__builtin_amdgcn_s_memrealtime() + (unsigned)(((w - 8) * PACEW) / 10);

  const unsigned voff = (unsigned)((l >> 3) * ROWB + (l & 7) * 16);
  const size_t kvoff = ((size_t)h * SK + (SK - WN) + (size_t)s * (WN / 8)) * DH;
  const __amdgpu_buffer_rsrc_t rk = mkrsrc(p.k + kvoff, (WN / 8) * ROWB);
  const __amdgpu_buffer_rsrc_t rv = mkrsrc(p.v + kvoff, (WN / 8) * ROWB);
  const int woff = w * (4 * GRPB);

  const bool proj = w < 10 && b < 240;
  const int row0 = (b & 1) * 320 + 32 * w;
  v4u K[12], V[12], A[4], X0, X1;
  if (__builtin_expect(proj, 1)) {
    const int ld = b < 192 ? HQ * DH : FF;
    const unsigned short* wb = b < 192 ? p.wq + (b >> 1) * 64 : p.w1 + ((b - 192) >> 1) * 64;
    const __amdgpu_buffer_rsrc_t ra = mkrsrc(wb + (size_t)row0 * ld, 32 * ld * 2);
    const __amdgpu_buffer_rsrc_t rx = mkrsrc(p.x, DM * 2);
    const unsigned aoff = (unsigned)((l >> 3) * ld * 2 + (l & 7) * 16);
    X0 = __builtin_amdgcn_raw_buffer_load_b128(rx, l * 16, 0, 0);
    X1 = __builtin_amdgcn_raw_buffer_load_b128(rx, l * 16 + 1024, 0, 0);
#pragma unroll
    for (int i = 0; i < 4; ++i) A[i] = __builtin_amdgcn_raw_buffer_load_b128(ra, aoff, i * 8 * ld * 2, W_AUX);
  }
  SB();
#pragma unroll
  for (int i = 0; i < WIN0; ++i) K[i] = KLOAD(i);
  SB();
  if (tid < 8) flags[tid] = 0;
  TS(12);
  BAR();
  TS(13);
  const u64 epv = get_gran(ws + G_EPOCH);
  SB();

  v4u mk;
  {
    const unsigned hm = (l & 1) ? 0xffff0000u : 0x0000ffffu;
    const int q4 = (l & 7) >> 1;
    mk.x = q4 == 0 ? hm : 0u; mk.y = q4 == 1 ? hm : 0u; mk.z = q4 == 2 ? hm : 0u; mk.w = q4 == 3 ? hm : 0u;
  }
  const bool valid = ((l >> 3) & 1) == (l >> 5);
  const int dcol = 8 * (l & 7) + 4 * ((l >> 4) & 1);
  const f32x4 zero4 = {0.f, 0.f, 0.f, 0.f};

  if (proj) {
    *(LAS v4u*)(lds + L_X + 4 * l) = X0;
    *(LAS v4u*)(lds + L_X + 256 + 4 * l) = X1;
    unsigned xr[4];
#pragma unroll
    for (int i = 0; i < 4; ++i) xr[i] = ((LAS unsigned short*)(lds + L_X))[row0 + 8 * i + (l >> 3)];
    float xs[16], sum = 0.f;
#pragma unroll
    for (int i = 0; i < 4; ++i) {
      xs[2 * i] = bf2f(X0[i] & 0xffffu); xs[2 * i + 1] = __uint_as_float(X0[i] & 0xffff0000u);
      xs[8 + 2 * i] = bf2f(X1[i] & 0xffffu); xs[8 + 2 * i + 1] = __uint_as_float(X1[i] & 0xffff0000u);
    }
#pragma unroll
    for (int i = 0; i < 8; ++i) sum += xs[i] + xs[8 + i];
    sum = xrow_sum(sum16(sum));
    const float mean = sum * (1.0f / DM);
    float sq = 0.f, sq1 = 0.f;
#pragma unroll
    for (int i = 0; i < 8; ++i) { const float d = xs[i] - mean, e = xs[8 + i] - mean; sq = fmaf(d, d, sq); sq1 = fmaf(e, e, sq1); }
    sq += l < 16 ? sq1 : 0.f;
    sq = xrow_sum(sum16(sq));
    const float sd = rbf(sqrtf(rbf(rbf(sq * (1.0f / DM)) + 1e-5f)));
    const float mb = rbf(mean);
    f32x4 d = zero4;
#pragma unroll
    for (int i = 0; i < 4; ++i) {
      const float xn = rbf(bf2f(xr[i]) - mb) / sd;
      d = mfma(mk & cvtpk(xn, xn), A[i], d);
    }
    if (valid) *(LAS f32x4*)(lds + L_QP + (2 * w + (l >> 5)) * QP_STR + dcol) = d;
    lds_signal(flags + 0, l);
    TS(1);
  }

  u64 tag;
#pragma unroll
  for (int i = WIN0; i < 12; ++i) {
    if ((i == PUBPOS + 1 || i == PUBPOS + 2) && w == 10) VMWAIT(WIN);
    else VMWAIT(WIN - 1);
    PACE(i);
    K[i] = KLOAD(i);
    SB();
    if (i == PUBPOS) {
      tag = ((u64)__builtin_amdgcn_readfirstlane((unsigned)(epv >> 32)) << 32 | (u64)__builtin_amdgcn_readfirstlane((unsigned)epv)) + 1;
      if (w == 10 && b < 240) {
        lds_wait_ge(flags + 0, 10);
        float acc = 0.f;
#pragma unroll
        for (int u = 0; u < 20; ++u) acc += lds[L_QP + u * QP_STR + l];
        put_gran(ws + G_Q + b * 64 + l, acc, (unsigned)tag, (unsigned)(tag >> 32) & 0xffu);
        TS(2);
      }
      SB();
    }
  }
  const unsigned tlo = (unsigned)tag, thi = (unsigned)(tag >> 32) & 0xffu;

  u64 g0 = 0, g1 = 0;
  unsigned b1v = 0;
  gu64* qsrc = ws + G_Q + (w < 15 ? 3 * h + (w - 12) : 96 + rb) * 128 + l;
  if (w >= 12) {
    g0 = get_gran(qsrc); g1 = get_gran(qsrc + 64);
    b1v = ((const GAS unsigned*)p.b1)[rb * 32 + (l >> 1)];
  }
  SB();

  float sc[4], mwg;
  v4u qf[3], sel[4], WO[2], W2;
  f32x4 acc[3] = {zero4, zero4, zero4};
  const unsigned oo = (unsigned)((l >> 3) * (DM * 2) + (l & 7) * 16);
  const __amdgpu_buffer_rsrc_t ro = mkrsrc(p.wo + (size_t)(256 * rb + 16 * w) * DM + 64 * cb, outp ? 16 * DM * 2 : 0);
  auto load_wo = [&](int lo, int hi, bool paced) {
#pragma unroll
    for (int u = lo; u < hi; ++u) {
      if (paced) VMWAIT(WIN - 1);
      if (u == 0) {
        const __amdgpu_buffer_rsrc_t r2 = mkrsrc(p.w2 + (size_t)(64 * rb + 8 * (w & 7)) * DM + 64 * cb, outp && w < 8 ? 8 * DM * 2 : 0);
        W2 = __builtin_amdgcn_raw_buffer_load_b128(r2, oo, 0, W_AUX);
      } else {
        WO[u - 1] = __builtin_amdgcn_raw_buffer_load_b128(ro, oo, (u - 1) * 8 * DM * 2, W_AUX);
      }
      SB();
    }
  };
  const int ha = (128 * rb) / 96;
  gu64* mlp = ws + G_O + (size_t)((ha + ((l >> 4) & 1)) * 8 + ((l >> 1) & 7)) * 128 + 96 + (l & 1);
  u64 gm = 0;
  const int d = (w & 1) * 64 + l, pr = 128 * rb + d;
  const int hh = pr / 96;
  gu64* og = ws + G_O + (size_t)(hh * 8) * 128 + (pr - hh * 96);
  const int fb = b - FB0;
  const bool fin = w == 10 && fb >= 0 && fb < 40 * FSTR && fb % FSTR == 0;
  const int fq = fb / FSTR;
  gu64* ys = ws + G_Y + (size_t)(((fq >> 2) & 15) * 24 + (l >> 4)) * 64 + 16 * (fq & 3) + (l & 15);
  unsigned tlbv = 0;
  unsigned ptv[8] = {0, 0, 0, 0, 0, 0, 0, 0};
#pragma unroll
  for (int i = 0; i < 12; ++i) {
    if (PT && (i == PT + 1 || i == PT + 2) && w >= 14) VMWAIT(WIN + 7);
    else if (PT && (i == PT + 1 || i == PT + 2) && w == 10) VMWAIT(WIN);
    else if (i >= EI) VMWAIT(WINE - 1);
    else if (i < WIN && w >= 12) VMWAIT(WIN + 2);
    else VMWAIT(WIN - 1);
    PACE(12 + i);
    V[i] = VLOAD(i);
    SB();
    if (MLPRE && i == 11 && w >= 14) gm = get_gran(mlp);
    if (PT && i == PT) {
      if (w >= 14) {
#pragma unroll
        for (int u = 0; u < 8; ++u) ptv[u] = __hip_atomic_load((GAS unsigned*)(og + u * 128), RLX_AGENT);
      } else if (fin) {
        ptv[0] = __hip_atomic_load((GAS unsigned*)ys, RLX_AGENT);
      }
      SB();
    }
    if (TLBT && i == TLBT && w == 9 && outp) {
      if (l < TLBN) tlbv = __builtin_nontemporal_load((const GAS unsigned*)(p.wo + (size_t)(256 * rb + (256 / TLBN) * l) * DM + 64 * cb));
      SB();
    }
    if (TLBT && i == TLBT + 3) asm volatile("" ::"v"(tlbv));
    if (PT && i == PT + 3) asm volatile("" ::"v"(ptv[0]), "v"(ptv[1]), "v"(ptv[2]), "v"(ptv[3]), "v"(ptv[4]), "v"(ptv[5]), "v"(ptv[6]), "v"(ptv[7]));
    if (i == GI && w >= 12) {
      if (__builtin_expect(!__all(gran_ok(g0, tlo, thi) & gran_ok(g1, tlo, thi)), 0)) {
#pragma clang loop unroll(disable)
        for (unsigned spins = 0; spins < SPIN_CAP; ++spins) {
          __builtin_amdgcn_s_sleep(4);
          g0 = get_gran(qsrc); g1 = get_gran(qsrc + 64);
          if (__all(gran_ok(g0, tlo, thi) & gran_ok(g1, tlo, thi))) break;
        }
      }
      const float tot = gran_val(g0) + gran_val(g1);
      if (w < 15) {
        ((LAS unsigned short*)(lds + L_QB))[(w - 12) * 64 + l] = (unsigned short)(cvtpk(tot, tot) & 0xffffu);
      } else {
        float hv = rbf(rbf(tot) + ((l & 1) ? __uint_as_float(b1v & 0xffff0000u) : bf2f(b1v & 0xffffu)));
        hv = hv > 0.f ? hv : 0.f;
        hv = hv * hv;
        ((LAS unsigned*)(lds + L_HB))[l] = cvtpk(hv, hv);
      }
      lds_signal(flags + 1, l);
      TS(3);
    }
    if (i == QI) {
      TS(4);
      lds_wait_ge(flags + 1, 4);
#pragma unroll
      for (int j = 0; j < 3; ++j) qf[j] = *(LAS v4u*)(lds + L_QB + 32 * j + 4 * (l & 7));
      TS(5);
      sc[0] = qk_score(K[0], K[1], K[2], qf);
      sc[1] = qk_score(K[3], K[4], K[5], qf);
    }
    if (i == QI + 1) {
      sc[2] = qk_score(K[6], K[7], K[8], qf);
      sc[3] = qk_score(K[9], K[10], K[11], qf);
      float mw = fmaxf(fmaxf(sc[0], sc[1]), fmaxf(sc[2], sc[3]));
      mw = xrow_max(fmaxf(mw, dpp<ROR8>(mw)));
      if (l == 0) lds[L_M + w] = mw;
      lds_signal(flags + 2, l);
    }
    if (i == QI + 3) {
      lds_wait_ge(flags + 2, 16);
      mwg = max16(lds[L_M + (l & 15)]);
      float lw = 0.f;
#pragma unroll
      for (int g = 0; g < 4; ++g) {
        const float pr = __builtin_amdgcn_exp2f((sc[g] - mwg) * LOG2E);
        lw += pr;
        sel[g] = mk & cvtpk(pr, pr);
      }
      lw = xrow_sum(lw + dpp<ROR8>(lw));
      if (l == 0) lds[L_L + w] = lw;
      lds_signal(flags + 4, l);
#pragma unroll
      for (int u = 0; u < 6; ++u) acc[u % 3] = mfma(sel[u / 3], V[u], acc[u % 3]);
      TS(6);
    }
    if (i == 10) {
#pragma unroll
      for (int u = 6; u < 9; ++u) acc[u % 3] = mfma(sel[2], V[u], acc[u % 3]);
    }
    SB();
    if (i == WOPOS) load_wo(0, WOSPLIT, true);
    if (i == QI + 5 && w == 11) {
      lds_wait_ge(flags + 4, 16);
      const float lsum = sum16(lds[L_L + (l & 15)]);
      if (l < 16) put_gran(ws + G_O + (size_t)b * 128 + 96 + l, l == 0 ? mwg : lsum, tlo, thi);
      SB();
    }
  }
  if (WOPACE) {
    PACET(PACEA + PACEB * 23 + PACEX + WOT0);
    load_wo(WOSPLIT, WOPACE == 1 ? 2 : 3, false);
  } else {
    lds_signal(flags + 3, l);
    if (WOIMM) {
      lds_wait_ge(flags + 3, WOIMM);
      load_wo(WOSPLIT, 3, false);
    }
  }
#pragma unroll
  for (int u = 9; u < 12; ++u) acc[u % 3] = mfma(sel[3], V[u], acc[u % 3]);
  {
    LAS float* op = lds + L_OP + (w + 16 * (l >> 5)) * OP_STR + dcol;
#pragma unroll
    for (int j = 0; j < 3; ++j) if (valid) *(LAS f32x4*)(op + 64 * j) = acc[j];
  }
  TS(7);
  if (!WOIMM && !WOPACE) {
    lds_wait_ge(flags + 3, 16);
    load_wo(WOSPLIT, 3, false);
  }
  TS(15);
  BAR();
  if (w < 6) {
    const int c4 = tid >> 3, pg = tid & 7;
    LAS float* src = lds + L_OP + pg * OP_STR + 4 * c4;
    f32x4 t = (*(LAS f32x4*)(src) + *(LAS f32x4*)(src + 8 * OP_STR)) + (*(LAS f32x4*)(src + 16 * OP_STR) + *(LAS f32x4*)(src + 24 * OP_STR));
#pragma unroll
    for (int r = 0; r < 4; ++r) t[r] = sum8(t[r]);
    const unsigned pk = (pg & 1) ? cvtpk(t[2], t[3]) : cvtpk(t[0], t[1]);
    if (pg < 2) put_gran2(ws + G_O + (size_t)b * 128 + 2 * c4 + pg, pk, tlo);
  }
  TS(8);
  if (WOPACE == 1) {
    PACET(PACEA + PACEB * 23 + PACEX + WOT1);
    load_wo(2, 3, false);
  }
  unsigned xfin = 0, b2fin = 0;
  if (fin) {
    xfin = ((const GAS unsigned*)p.x)[8 * fq + ((l & 15) >> 1)];
    b2fin = ((const GAS unsigned*)p.b2)[8 * fq + ((l & 15) >> 1)];
  }
  asm volatile("" : "+v"(ys));
  const int yc4 = l >> 2, ypg = l & 3;
  unsigned zb = 0;
  if (outp) {
    LAS unsigned* osrc = (LAS unsigned*)(lds + L_OB) + 16 * w + (l >> 3);
    LAS float* ydst = lds + L_YP + (2 * w + (l >> 5)) * YP_STR + dcol;
    asm volatile("" : "+v"(osrc), "+v"(ydst));
    LAS float* ysrc = lds + L_YP + ypg * YP_STR + 4 * yc4;
    gu64* ydg = ws + G_Y + (size_t)(cb * 24 + rb) * 64 + l;
    asm volatile("" : "+v"(ysrc), "+v"(ydg));
    if (w >= 14) {
      LAS v2u* odst = (LAS v2u*)(lds + L_OB + 2 * d);
      asm volatile("" : "+v"(odst));
      u64 g[8];
      float fi[8];
      if (!__all(gran_ok(gm, tlo, thi))) {
#pragma clang loop unroll(disable)
        for (unsigned spins = 0; spins < SPIN_CAP; ++spins) {
          gm = get_gran(mlp);
          if (__all(gran_ok(gm, tlo, thi))) break;
          __builtin_amdgcn_s_sleep(2);
        }
      }
      {
        const float ml = gran_val(gm);
        const float mo = dpp<XOR1>(ml);
        const float ms = (l & 1) ? mo : ml, ls = (l & 1) ? ml : mo;
        const float M = max16(ms);
        const float e = __builtin_amdgcn_exp2f((ms - M) * LOG2E);
        const float f = e * __builtin_amdgcn_rcpf(0.5f * sum16(e * ls));
#pragma unroll
        for (int i = 0; i < 8; ++i) {
          const float fa = __builtin_bit_cast(float, __builtin_amdgcn_readlane(__builtin_bit_cast(int, f), 2 * i));
          const float fb = __builtin_bit_cast(float, __builtin_amdgcn_readlane(__builtin_bit_cast(int, f), 16 + 2 * i));
          fi[i] = hh == ha ? fa : fb;
          asm volatile("" : "+v"(fi[i]));
        }
      }
      if (GDELAY) __builtin_amdgcn_s_sleep(GDELAY);
      float o0, o1;
      for (unsigned spins = 0;; ++spins) {
#pragma unroll
        for (int i = 0; i < 8; ++i) g[i] = get_gran(og + i * 128);
        SB();
        unsigned bad = 0;
        o0 = 0.f; o1 = 0.f;
#pragma unroll
        for (int i = 0; i < 8; ++i) {
          const unsigned v = (unsigned)(g[i] >> 32);
          bad |= (unsigned)g[i] ^ tlo;
          o0 = fmaf(fi[i], bf2f(v & 0xffffu), o0);
          o1 = fmaf(fi[i], __uint_as_float(v & 0xffff0000u), o1);
          asm volatile("" : "+v"(o0), "+v"(o1), "+v"(bad));
        }
        if (__all(bad == 0) || spins > SPIN_CAP) break;
        if (GSLEEP) __builtin_amdgcn_s_sleep(GSLEEP);
      }
      *odst = v2u{cvtpk(o0, o0), cvtpk(o1, o1)};
    } else if (w < 8) {
      const f32x4 z = mfma(mk & ((LAS unsigned*)(lds + L_HB))[8 * w + (l >> 3)], W2, zero4);
      if (valid) *(LAS f32x4*)(lds + L_ZP + (2 * w + (l >> 5)) * YP_STR + dcol) = z;
      lds_signal(flags + 5, l);
      if (w == 0) {
        lds_wait_ge(flags + 5, 8);
        LAS float* src = lds + L_ZP + ypg * YP_STR + 4 * yc4;
        f32x4 t = (*(LAS f32x4*)(src) + *(LAS f32x4*)(src + 4 * YP_STR)) + (*(LAS f32x4*)(src + 8 * YP_STR) + *(LAS f32x4*)(src + 12 * YP_STR));
#pragma unroll
        for (int r = 0; r < 4; ++r) { t[r] += dpp<XOR1>(t[r]); t[r] += dpp<XOR2>(t[r]); }
        zb = cvtpk(0.f, (ypg & 2) ? ((ypg & 1) ? t[3] : t[2]) : ((ypg & 1) ? t[1] : t[0]));
        asm volatile("" : "+v"(zb));
      }
    }
    if (DIAGACK && w > 0 && w < 14) { VMWAIT(0); TS(2); }
    TS(9);
    BAR();
    {
      const unsigned o0 = osrc[0], o1 = osrc[8];
      f32x4 y = mfma(mk & o0, WO[0], zero4);
      y = mfma(mk & o1, WO[1], y);
      if (valid) *(LAS f32x4*)ydst = y;
    }
    BAR();
    if (w == 0) {
      f32x4 t[8];
#pragma unroll
      for (int u = 0; u < 8; ++u) t[u] = *(LAS f32x4*)(ysrc + 4 * u * YP_STR);
      f32x4 a = ((t[0] + t[1]) + (t[2] + t[3])) + ((t[4] + t[5]) + (t[6] + t[7]));
#pragma unroll
      for (int r = 0; r < 4; ++r) { a[r] += dpp<XOR1>(a[r]); a[r] += dpp<XOR2>(a[r]); }
      const float ys = (ypg & 2) ? ((ypg & 1) ? a[3] : a[2]) : ((ypg & 1) ? a[1] : a[0]);
      put_gran2(ydg, (cvtpk(ys, ys) & 0xffffu) | zb, tlo);
      TS(10);
      if (DIAGACK) { VMWAIT(0); TS(3); }
    }
  }
  if (fin) {
    float sy, sz;
    if (FDELAY) __builtin_amdgcn_s_sleep(FDELAY);
    for (unsigned spins = 0;; ++spins) {
      u64 g[6];
#pragma unroll
      for (int u = 0; u < 6; ++u) g[u] = get_gran(ys + u * 4 * 64);
      SB();
      unsigned bad = 0;
      sy = 0.f; sz = 0.f;
#pragma unroll
      for (int u = 0; u < 6; ++u) {
        const unsigned v = (unsigned)(g[u] >> 32);
        bad |= (unsigned)g[u] ^ tlo;
        sy += bf2f(v & 0xffffu); sz += __uint_as_float(v & 0xffff0000u);
        asm volatile("" : "+v"(sy), "+v"(sz), "+v"(bad));
      }
      if (__all(bad == 0) || spins > SPIN_CAP) break;
      if (FSLEEP) __builtin_amdgcn_s_sleep(FSLEEP);
    }
    sy = xrow_sum(sy); sz = xrow_sum(sz);
    const float bb = (l & 1) ? __uint_as_float(b2fin & 0xffff0000u) : bf2f(b2fin & 0xffffu);
    const float xx = (l & 1) ? __uint_as_float(xfin & 0xffff0000u) : bf2f(xfin & 0xffffu);
    const float o2 = rbf(rbf(sz) + bb);
    const float gt = rbf(o2 * __builtin_amdgcn_rcpf(1.0f + __builtin_amdgcn_exp2f(-o2 * LOG2E)));
    const float res = xx + rbf(rbf(sy) * gt);
    const unsigned pk = cvtpk(res, dpp<XOR1>(res));
    if (l < 16 && !(l & 1)) ((GAS unsigned*)p.out)[8 * fq + (l >> 1)] = pk;
    if (fb == 0 && l == 0) __hip_atomic_store(ws + G_EPOCH, tag, RLX_AGENT);
    TS(11);
  }
}

static inline size_t ws_offset(size_t) { return 0; }

extern "C" void kernel_launch(void* const* d_in, const int* in_sizes, int n_in,
                              void* d_out, int out_size, void* d_ws, size_t ws_size,
                              hipStream_t stream) {
  Params p;
  p.x = (const unsigned short*)d_in[0];
  p.k = (const unsigned short*)d_in[1];
  p.v = (const unsigned short*)d_in[2];
  p.wq = (const unsigned short*)d_in[3];
  p.wo = (const unsigned short*)d_in[4];
  p.w1 = (const unsigned short*)d_in[6];
  p.b1 = (const unsigned short*)d_in[7];
  p.w2 = (const unsigned short*)d_in[8];
  p.b2 = (const unsigned short*)d_in[9];
  p.out = (unsigned short*)d_out;
  p.ws = (u64*)((char*)d_ws + ws_offset(ws_size));
  hipLaunchKernelGGL(fused_decode, dim3(NWG), dim3(NT), 0, stream, p);
}
```

```cpp
#define PROBE 0
#define PACEB 0
#define WOPACE 0
#include <hip/hip_runtime.h>
#include <hip/hip_bf16.h>
#include <stdint.h>

typedef unsigned v4u __attribute__((ext_vector_type(4)));
typedef unsigned v2u __attribute__((ext_vector_type(2)));
typedef float f32x4 __attribute__((ext_vector_type(4)));
typedef short bf16x8 __attribute__((ext_vector_type(8)));
typedef __bf16 bf16x2_t __attribute__((ext_vector_type(2)));
typedef unsigned long long u64;
#define GAS __attribute__((address_space(1)))
#define LAS __attribute__((address_space(3)))
#define RLX_AGENT __ATOMIC_RELAXED, __HIP_MEMORY_SCOPE_AGENT
#define RLX_WG __ATOMIC_RELAXED, __HIP_MEMORY_SCOPE_WORKGROUP
typedef GAS u64 gu64;

#define WIN 2
#define WIN0 2
#define QI 4
#define GI 1
#define EI 9
#define WINE 4
#define PUBPOS 5
#define WOPOS 8
#define WOSPLIT 1
#define WOIMM 0
#define GDELAY 0
#define FB0 16
#define GSLEEP 0
#define FSLEEP 0
#define FDELAY 0
#define F128 0
#define MLPRE 1
#define PACEA 900
#define PACEW 0
#define PACEX 0
#define PT 0
#define WOT0 600
#define WOT1 1200
#define TLBT 0
#define TLBN 8
#define FW16 1
#define FSTR 5
#define DIAGACK 0
#define K_AUX 2
#define V_AUX 2
#define W_AUX 2

constexpr int DM = 640, HQ = 32, DH = 192, SK = 32768, WN = 4096, FF = 1536;
constexpr int NWG = 256, NT = 1024;
constexpr int ROWB = DH * 2;
constexpr int GRPB = 8 * ROWB;
constexpr float LOG2E = 1.4426950408889634f;
constexpr int RSRC_W3 = 0x00020000;
constexpr unsigned SPIN_CAP = 1u << 18;

constexpr size_t G_EPOCH = 0;
constexpr size_t G_Q = 64;
constexpr size_t G_O = G_Q + 256 * 64;
constexpr size_t G_Y = G_O + (size_t)NWG * 128;
constexpr size_t G_END = G_Y + (size_t)240 * 64;
constexpr size_t G_TS = G_END + 64;

constexpr int QP_STR = 68;
constexpr int OP_STR = 200;
constexpr int YP_STR = 80;
constexpr int L_QP = 0;
constexpr int L_OP = L_QP + 20 * QP_STR;
constexpr int L_YP = L_OP + 32 * OP_STR;
constexpr int L_ZP = L_YP + 32 * YP_STR;
constexpr int L_X = L_ZP + 16 * YP_STR;
constexpr int L_QB = L_X + 512;
constexpr int L_OB = L_QB + 96;
constexpr int L_HB = L_OB + 256;
constexpr int L_M = L_HB + 64;
constexpr int L_L = L_M + 16;
constexpr int L_FLAG = L_L + 16;
constexpr int L_END = L_FLAG + 8;

struct Params {
  const unsigned short* x;
  const unsigned short* k;
  const unsigned short* v;
  const unsigned short* wq;
  const unsigned short* wo;
  const unsigned short* w1;
  const unsigned short* b1;
  const unsigned short* w2;
  const unsigned short* b2;
  unsigned short* out;
  u64* ws;
};

__device__ __forceinline__ float bf2f(unsigned h) { return __builtin_bit_cast(float, h << 16); }
__device__ __forceinline__ unsigned cvtpk(float lo, float hi) {
  bf16x2_t r = {(__bf16)lo, (__bf16)hi};
  return __builtin_bit_cast(unsigned, r);
}
__device__ __forceinline__ float rbf(float f) { return bf2f(cvtpk(f, f) & 0xffffu); }
__device__ __forceinline__ float dot2(unsigned a, unsigned b, float acc) {
  return __builtin_amdgcn_fdot2_f32_bf16(__builtin_bit_cast(bf16x2_t, a), __builtin_bit_cast(bf16x2_t, b), acc, false);
}
template <int CTRL> __device__ __forceinline__ float dpp(float x) {
  return __builtin_bit_cast(float, __builtin_amdgcn_mov_dpp(__builtin_bit_cast(int, x), CTRL, 0xf, 0xf, true));
}
constexpr int XOR1 = 0xB1, XOR2 = 0x4E, HMIR = 0x141, ROR8 = 0x128;
__device__ __forceinline__ float sum8(float x) { x += dpp<XOR1>(x); x += dpp<XOR2>(x); x += dpp<HMIR>(x); return x; }
__device__ __forceinline__ float sum16(float x) { x = sum8(x); x += dpp<ROR8>(x); return x; }
__device__ __forceinline__ float max16(float x) {
  x = fmaxf(x, dpp<XOR1>(x)); x = fmaxf(x, dpp<XOR2>(x)); x = fmaxf(x, dpp<HMIR>(x)); x = fmaxf(x, dpp<ROR8>(x)); return x; }
__device__ __forceinline__ float xrow_sum(float x) {
  auto s = __builtin_amdgcn_permlane16_swap(__float_as_uint(x), __float_as_uint(x), false, false);
  x = __uint_as_float(s[0]) + __uint_as_float(s[1]);
  auto t = __builtin_amdgcn_permlane32_swap(__float_as_uint(x), __float_as_uint(x), false, false);
  return __uint_as_float(t[0]) + __uint_as_float(t[1]);
}
__device__ __forceinline__ float xrow_max(float x) {
  auto s = __builtin_amdgcn_permlane16_swap(__float_as_uint(x), __float_as_uint(x), false, false);
  x = fmaxf(__uint_as_float(s[0]), __uint_as_float(s[1]));
  auto t = __builtin_amdgcn_permlane32_swap(__float_as_uint(x), __float_as_uint(x), false, false);
  return fmaxf(__uint_as_float(t[0]), __uint_as_float(t[1]));
}
__device__ __forceinline__ f32x4 mfma(v4u a, v4u b, f32x4 c) {
  return __builtin_amdgcn_mfma_f32_16x16x32_bf16(__builtin_bit_cast(bf16x8, a), __builtin_bit_cast(bf16x8, b), c, 0, 0, 0);
}
__device__ __forceinline__ __amdgpu_buffer_rsrc_t mkrsrc(const void* p, unsigned bytes) {
  return __builtin_amdgcn_make_buffer_rsrc((void*)p, 0, bytes, RSRC_W3);
}
#define SB() __builtin_amdgcn_sched_barrier(0)
#define VMWAIT(n) asm volatile("s_waitcnt vmcnt(%0)" ::"n"(n) : "memory")
#define LGKM0() asm volatile("s_waitcnt lgkmcnt(0)" ::: "memory")
#define BAR() do { LGKM0(); __builtin_amdgcn_s_barrier(); asm volatile("" ::: "memory"); } while (0)

__device__ __forceinline__ void put_gran(gu64* g, float v, unsigned tlo, unsigned thi) {
  const unsigned hi = (__float_as_uint(v) & 0xffffff00u) | thi;
  __hip_atomic_store(g, ((u64)hi << 32) | tlo, RLX_AGENT);
}
__device__ __forceinline__ bool gran_ok(u64 g, unsigned tlo, unsigned thi) {
  return ((unsigned)g == tlo) & (((unsigned)(g >> 32) & 0xffu) == thi);
}
__device__ __forceinline__ float gran_val(u64 g) { return __uint_as_float((unsigned)(g >> 32) & 0xffffff00u); }
__device__ __forceinline__ void put_gran2(gu64* g, unsigned v, unsigned tlo) { __hip_atomic_store(g, ((u64)v << 32) | tlo, RLX_AGENT); }
__device__ __forceinline__ u64 get_gran(gu64* g) { return __hip_atomic_load(g, RLX_AGENT); }

__device__ __forceinline__ void lds_wait_ge(LAS unsigned* f, unsigned n) {
  while (__hip_atomic_load(f, RLX_WG) < n) __builtin_amdgcn_s_sleep(1);
  asm volatile("" ::: "memory");
}
__device__ __forceinline__ int opaque0() { int z; asm("v_mov_b32 %0, 0" : "=v"(z)); return z; }
__device__ __forceinline__ void lds_signal(LAS unsigned* f, int lane) {
  LGKM0();
  if (lane == 0) (void)__hip_atomic_fetch_add(f + opaque0(), 1u, RLX_WG);
}
__device__ __forceinline__ float qk_score(v4u k0, v4u k1, v4u k2, const v4u (&qf)[3]) {
  float d0 = dot2(k0.x, qf[0].x, 0.f), d1 = dot2(k0.y, qf[0].y, 0.f);
  d0 = dot2(k0.z, qf[0].z, d0); d1 = dot2(k0.w, qf[0].w, d1);
  d0 = dot2(k1.x, qf[1].x, d0); d1 = dot2(k1.y, qf[1].y, d1);
  d0 = dot2(k1.z, qf[1].z, d0); d1 = dot2(k1.w, qf[1].w, d1);
  d0 = dot2(k2.x, qf[2].x, d0); d1 = dot2(k2.y, qf[2].y, d1);
  d0 = dot2(k2.z, qf[2].z, d0); d1 = dot2(k2.w, qf[2].w, d1);
  return rbf(rbf(sum8(d0 + d1)) * 0.07216878364870322f);
}
#define PACE(j) do {} while (0)
#define PACET(t) do {} while (0)
#define KLOAD(i) __builtin_amdgcn_raw_buffer_load_b128(rk, voff + ((i) % 3) * 128, woff + ((i) / 3) * GRPB, K_AUX)
#define VLOAD(i) __builtin_amdgcn_raw_buffer_load_b128(rv, voff + ((i) % 3) * 128, woff + ((i) / 3) * GRPB, V_AUX)

#define TS(i) do { if ((i) == PROBE) { SB(); tpr = __builtin_amdgcn_s_memrealtime(); SB(); } } while (0)

__global__ void __launch_bounds__(NT) fused_decode(Params p) {
  __shared__ __attribute__((aligned(16))) float lds_[L_END];
  LAS float* lds = (LAS float*)lds_;
  LAS unsigned* flags = (LAS unsigned*)(lds + L_FLAG);
  const int tid = threadIdx.x, l = tid & 63, w = __builtin_amdgcn_readfirstlane(tid >> 6);
  const int b = blockIdx.x;
  const int s = b & 7, h = b >> 3;
  const bool outp = b >= 16;
  const int rb = outp ? (b - 16) / 10 : 0, cb = outp ? (b - 16) % 10 : 0;
  u64 tpr = 0;
  TS(0);
  gu64* ws = (gu64*)p.ws;

  const unsigned voff = (unsigned)((l >> 3) * ROWB + (l & 7) * 16);
  const size_t kvoff = ((size_t)h * SK + (SK - WN) + (size_t)s * (WN / 8)) * DH;
  const __amdgpu_buffer_rsrc_t rk = mkrsrc(p.k + kvoff, (WN / 8) * ROWB);
  const __amdgpu_buffer_rsrc_t rv = mkrsrc(p.v + kvoff, (WN / 8) * ROWB);
  const int woff = w * (4 * GRPB);

  const bool proj = w < 10 && b < 240;
  const int row0 = (b & 1) * 320 + 32 * w;
  v4u K[12], V[12], A[4], X0, X1;
  if (__builtin_expect(proj, 1)) {
    const int ld = b < 192 ? HQ * DH : FF;
    const unsigned short* wb = b < 192 ? p.wq + (b >> 1) * 64 : p.w1 + ((b - 192) >> 1) * 64;
    const __amdgpu_buffer_rsrc_t ra = mkrsrc(wb + (size_t)row0 * ld, 32 * ld * 2);
    const __amdgpu_buffer_rsrc_t rx = mkrsrc(p.x, DM * 2);
    const unsigned aoff = (unsigned)((l >> 3) * ld * 2 + (l & 7) * 16);
    X0 = __builtin_amdgcn_raw_buffer_load_b128(rx, l * 16, 0, 0);
    X1 = __builtin_amdgcn_raw_buffer_load_b128(rx, l * 16 + 1024, 0, 0);
#pragma unroll
    for (int i = 0; i < 4; ++i) A[i] = __builtin_amdgcn_raw_buffer_load_b128(ra, aoff, i * 8 * ld * 2, W_AUX);
  }
  SB();
#pragma unroll
  for (int i = 0; i < WIN0; ++i) K[i] = KLOAD(i);
  SB();
  if (tid < 8) flags[tid] = 0;
  TS(12);
  BAR();
  TS(13);
  const u64 epv = get_gran(ws + G_EPOCH);
  SB();

  v4u mk;
  {
    const unsigned hm = (l & 1) ? 0xffff0000u : 0x0000ffffu;
    const int q4 = (l & 7) >> 1;
    mk.x = q4 == 0 ? hm : 0u; mk.y = q4 == 1 ? hm : 0u; mk.z = q4 == 2 ? hm : 0u; mk.w = q4 == 3 ? hm : 0u;
  }
  const bool valid = ((l >> 3) & 1) == (l >> 5);
  const int dcol = 8 * (l & 7) + 4 * ((l >> 4) & 1);
  const f32x4 zero4 = {0.f, 0.f, 0.f, 0.f};

  if (proj) {
    *(LAS v4u*)(lds + L_X + 4 * l) = X0;
    *(LAS v4u*)(lds + L_X + 256 + 4 * l) = X1;
    unsigned xr[4];
#pragma unroll
    for (int i = 0; i < 4; ++i) xr[i] = ((LAS unsigned short*)(lds + L_X))[row0 + 8 * i + (l >> 3)];
    float xs[16], sum = 0.f;
#pragma unroll
    for (int i = 0; i < 4; ++i) {
      xs[2 * i] = bf2f(X0[i] & 0xffffu); xs[2 * i + 1] = __uint_as_float(X0[i] & 0xffff0000u);
      xs[8 + 2 * i] = bf2f(X1[i] & 0xffffu); xs[8 + 2 * i + 1] = __uint_as_float(X1[i] & 0xffff0000u);
    }
#pragma unroll
    for (int i = 0; i < 8; ++i) sum += xs[i] + xs[8 + i];
    sum = xrow_sum(sum16(sum));
    const float mean = sum * (1.0f / DM);
    float sq = 0.f, sq1 = 0.f;
#pragma unroll
    for (int i = 0; i < 8; ++i) { const float d = xs[i] - mean, e = xs[8 + i] - mean; sq = fmaf(d, d, sq); sq1 = fmaf(e, e, sq1); }
    sq += l < 16 ? sq1 : 0.f;
    sq = xrow_sum(sum16(sq));
    const float sd = rbf(sqrtf(rbf(rbf(sq * (1.0f / DM)) + 1e-5f)));
    const float mb = rbf(mean);
    f32x4 d = zero4;
#pragma unroll
    for (int i = 0; i < 4; ++i) {
      const float xn = rbf(bf2f(xr[i]) - mb) / sd;
      d = mfma(mk & cvtpk(xn, xn), A[i], d);
    }
    if (valid) *(LAS f32x4*)(lds + L_QP + (2 * w + (l >> 5)) * QP_STR + dcol) = d;
    lds_signal(flags + 0, l);
    TS(1);
  }

  u64 tag;
#pragma unroll
  for (int i = WIN0; i < 12; ++i) {
    if ((i == PUBPOS + 1 || i == PUBPOS + 2) && w == 10) VMWAIT(WIN);
    else VMWAIT(WIN - 1);
    PACE(i);
    K[i] = KLOAD(i);
    SB();
    if (i == 3) TS(14);
    if (i == PUBPOS) {
      tag = ((u64)__builtin_amdgcn_readfirstlane((unsigned)(epv >> 32)) << 32 | (u64)__builtin_amdgcn_readfirstlane((unsigned)epv)) + 1;
      if (w == 10 && b < 240) {
        lds_wait_ge(flags + 0, 10);
        float acc = 0.f;
#pragma unroll
        for (int u = 0; u < 20; ++u) acc += lds[L_QP + u * QP_STR + l];
        put_gran(ws + G_Q + b * 64 + l, acc, (unsigned)tag, (unsigned)(tag >> 32) & 0xffu);
        TS(2);
      }
      SB();
    }
  }
  const unsigned tlo = (unsigned)tag, thi = (unsigned)(tag >> 32) & 0xffu;

  u64 g0 = 0, g1 = 0;
  unsigned b1v = 0;
  gu64* qsrc = ws + G_Q + (w < 15 ? 3 * h + (w - 12) : 96 + rb) * 128 + l;
  if (w >= 12) {
    g0 = get_gran(qsrc); g1 = get_gran(qsrc + 64);
    b1v = ((const GAS unsigned*)p.b1)[rb * 32 + (l >> 1)];
  }
  SB();

  float sc[4], mwg;
  v4u qf[3], sel[4], WO[2], W2;
  f32x4 acc[3] = {zero4, zero4, zero4};
  const unsigned oo = (unsigned)((l >> 3) * (DM * 2) + (l & 7) * 16);
  const __amdgpu_buffer_rsrc_t ro = mkrsrc(p.wo + (size_t)(256 * rb + 16 * w) * DM + 64 * cb, outp ? 16 * DM * 2 : 0);
  auto load_wo = [&](int lo, int hi, bool paced) {
#pragma unroll
    for (int u = lo; u < hi; ++u) {
      if (paced) VMWAIT(WIN - 1);
      if (u == 0) {
        const __amdgpu_buffer_rsrc_t r2 = mkrsrc(p.w2 + (size_t)(64 * rb + 8 * (w & 7)) * DM + 64 * cb, outp && w < 8 ? 8 * DM * 2 : 0);
        W2 = __builtin_amdgcn_raw_buffer_load_b128(r2, oo, 0, W_AUX);
      } else {
        WO[u - 1] = __builtin_amdgcn_raw_buffer_load_b128(ro, oo, (u - 1) * 8 * DM * 2, W_AUX);
      }
      SB();
    }
  };
  const int ha = (128 * rb) / 96;
  gu64* mlp = ws + G_O + (size_t)((ha + ((l >> 4) & 1)) * 8 + ((l >> 1) & 7)) * 128 + 96 + (l & 1);
  u64 gm = 0;
  const int d = (w & 1) * 64 + l, pr = 128 * rb + d;
  const int hh = pr / 96;
  gu64* og = ws + G_O + (size_t)(hh * 8) * 128 + (pr - hh * 96);
  const int fb = b - FB0;
  const bool fin = w == 10 && fb >= 0 && fb < 40 * FSTR && fb % FSTR == 0;
  const int fq = fb / FSTR;
  gu64* ys = ws + G_Y + (size_t)(((fq >> 2) & 15) * 24 + (l >> 4)) * 64 + 16 * (fq & 3) + (l & 15);
  unsigned tlbv = 0;
  unsigned ptv[8] = {0, 0, 0, 0, 0, 0, 0, 0};
#pragma unroll
  for (int i = 0; i < 12; ++i) {
    if (PT && (i == PT + 1 || i == PT + 2) && w >= 14) VMWAIT(WIN + 7);
    else if (PT && (i == PT + 1 || i == PT + 2) && w == 10) VMWAIT(WIN);
    else if (i >= EI) VMWAIT(WINE - 1);
    else if (i < WIN && w >= 12) VMWAIT(WIN + 2);
    else VMWAIT(WIN - 1);
    PACE(12 + i);
    V[i] = VLOAD(i);
    SB();
    if (MLPRE && i == 11 && w >= 14) gm = get_gran(mlp);
    if (PT && i == PT) {
      if (w >= 14) {
#pragma unroll
        for (int u = 0; u < 8; ++u) ptv[u] = __hip_atomic_load((GAS unsigned*)(og + u * 128), RLX_AGENT);
      } else if (fin) {
        ptv[0] = __hip_atomic_load((GAS unsigned*)ys, RLX_AGENT);
      }
      SB();
    }
    if (TLBT && i == TLBT && w == 9 && outp) {
      if (l < TLBN) tlbv = __builtin_nontemporal_load((const GAS unsigned*)(p.wo + (size_t)(256 * rb + (256 / TLBN) * l) * DM + 64 * cb));
      SB();
    }
    if (TLBT && i == TLBT + 3) asm volatile("" ::"v"(tlbv));
    if (PT && i == PT + 3) asm volatile("" ::"v"(ptv[0]), "v"(ptv[1]), "v"(ptv[2]), "v"(ptv[3]), "v"(ptv[4]), "v"(ptv[5]), "v"(ptv[6]), "v"(ptv[7]));
    if (i == GI && w >= 12) {
      if (__builtin_expect(!__all(gran_ok(g0, tlo, thi) & gran_ok(g1, tlo, thi)), 0)) {
#pragma clang loop unroll(disable)
        for (unsigned spins = 0; spins < SPIN_CAP; ++spins) {
          __builtin_amdgcn_s_sleep(4);
          g0 = get_gran(qsrc); g1 = get_gran(qsrc + 64);
          if (__all(gran_ok(g0, tlo, thi) & gran_ok(g1, tlo, thi))) break;
        }
      }
      const float tot = gran_val(g0) + gran_val(g1);
      if (w < 15) {
        ((LAS unsigned short*)(lds + L_QB))[(w - 12) * 64 + l] = (unsigned short)(cvtpk(tot, tot) & 0xffffu);
      } else {
        float hv = rbf(rbf(tot) + ((l & 1) ? __uint_as_float(b1v & 0xffff0000u) : bf2f(b1v & 0xffffu)));
        hv = hv > 0.f ? hv : 0.f;
        hv = hv * hv;
        ((LAS unsigned*)(lds + L_HB))[l] = cvtpk(hv, hv);
      }
      lds_signal(flags + 1, l);
      TS(3);
    }
    if (i == QI) {
      TS(4);
      lds_wait_ge(flags + 1, 4);
#pragma unroll
      for (int j = 0; j < 3; ++j) qf[j] = *(LAS v4u*)(lds + L_QB + 32 * j + 4 * (l & 7));
      TS(5);
      sc[0] = qk_score(K[0], K[1], K[2], qf);
      sc[1] = qk_score(K[3], K[4], K[5], qf);
    }
    if (i == QI + 1) {
      sc[2] = qk_score(K[6], K[7], K[8], qf);
      sc[3] = qk_score(K[9], K[10], K[11], qf);
      float mw = fmaxf(fmaxf(sc[0], sc[1]), fmaxf(sc[2], sc[3]));
      mw = xrow_max(fmaxf(mw, dpp<ROR8>(mw)));
      if (l == 0) lds[L_M + w] = mw;
      lds_signal(flags + 2, l);
    }
    if (i == QI + 3) {
      lds_wait_ge(flags + 2, 16);
      mwg = max16(lds[L_M + (l & 15)]);
      float lw = 0.f;
#pragma unroll
      for (int g = 0; g < 4; ++g) {
        const float pr = __builtin_amdgcn_exp2f((sc[g] - mwg) * LOG2E);
        lw += pr;
        sel[g] = mk & cvtpk(pr, pr);
      }
      lw = xrow_sum(lw + dpp<ROR8>(lw));
      if (l == 0) lds[L_L + w] = lw;
      lds_signal(flags + 4, l);
#pragma unroll
      for (int u = 0; u < 6; ++u) acc[u % 3] = mfma(sel[u / 3], V[u], acc[u % 3]);
      TS(6);
    }
    if (i == 10) {
#pragma unroll
      for (int u = 6; u < 9; ++u) acc[u % 3] = mfma(sel[2], V[u], acc[u % 3]);
    }
    SB();
    if (i == WOPOS) load_wo(0, WOSPLIT, true);
    if (i == QI + 5 && w == 11) {
      lds_wait_ge(flags + 4, 16);
      const float lsum = sum16(lds[L_L + (l & 15)]);
      if (l < 16) put_gran(ws + G_O + (size_t)b * 128 + 96 + l, l == 0 ? mwg : lsum, tlo, thi);
      SB();
    }
  }
  if (WOPACE) {
    PACET(PACEA + PACEB * 23 + PACEX + WOT0);
    load_wo(WOSPLIT, WOPACE == 1 ? 2 : 3, false);
  } else {
    lds_signal(flags + 3, l);
    if (WOIMM) {
      lds_wait_ge(flags + 3, WOIMM);
      load_wo(WOSPLIT, 3, false);
    }
  }
#pragma unroll
  for (int u = 9; u < 12; ++u) acc[u % 3] = mfma(sel[3], V[u], acc[u % 3]);
  {
    LAS float* op = lds + L_OP + (w + 16 * (l >> 5)) * OP_STR + dcol;
#pragma unroll
    for (int j = 0; j < 3; ++j) if (valid) *(LAS f32x4*)(op + 64 * j) = acc[j];
  }
  TS(7);
  if (!WOIMM && !WOPACE) {
    lds_wait_ge(flags + 3, 16);
    load_wo(WOSPLIT, 3, false);
  }
  TS(15);
  BAR();
  if (w < 6) {
    const int c4 = tid >> 3, pg = tid & 7;
    LAS float* src = lds + L_OP + pg * OP_STR + 4 * c4;
    f32x4 t = (*(LAS f32x4*)(src) + *(LAS f32x4*)(src + 8 * OP_STR)) + (*(LAS f32x4*)(src + 16 * OP_STR) + *(LAS f32x4*)(src + 24 * OP_STR));
#pragma unroll
    for (int r = 0; r < 4; ++r) t[r] = sum8(t[r]);
    const unsigned pk = (pg & 1) ? cvtpk(t[2], t[3]) : cvtpk(t[0], t[1]);
    if (pg < 2) put_gran2(ws + G_O + (size_t)b * 128 + 2 * c4 + pg, pk, tlo);
  }
  TS(8);
  if (WOPACE == 1) {
    PACET(PACEA + PACEB * 23 + PACEX + WOT1);
    load_wo(2, 3, false);
  }
  unsigned xfin = 0, b2fin = 0;
  if (fin) {
    xfin = ((const GAS unsigned*)p.x)[8 * fq + ((l & 15) >> 1)];
    b2fin = ((const GAS unsigned*)p.b2)[8 * fq + ((l & 15) >> 1)];
  }
  asm volatile("" : "+v"(ys));
  const int yc4 = l >> 2, ypg = l & 3;
  unsigned zb = 0;
  if (outp) {
    LAS unsigned* osrc = (LAS unsigned*)(lds + L_OB) + 16 * w + (l >> 3);
    LAS float* ydst = lds + L_YP + (2 * w + (l >> 5)) * YP_STR + dcol;
    asm volatile("" : "+v"(osrc), "+v"(ydst));
    LAS float* ysrc = lds + L_YP + ypg * YP_STR + 4 * yc4;
    gu64* ydg = ws + G_Y + (size_t)(cb * 24 + rb) * 64 + l;
    asm volatile("" : "+v"(ysrc), "+v"(ydg));
    if (w >= 14) {
      LAS v2u* odst = (LAS v2u*)(lds + L_OB + 2 * d);
      asm volatile("" : "+v"(odst));
      u64 g[8];
      float fi[8];
      if (!__all(gran_ok(gm, tlo, thi))) {
#pragma clang loop unroll(disable)
        for (unsigned spins = 0; spins < SPIN_CAP; ++spins) {
          gm = get_gran(mlp);
          if (__all(gran_ok(gm, tlo, thi))) break;
          __builtin_amdgcn_s_sleep(2);
        }
      }
      {
        const float ml = gran_val(gm);
        const float mo = dpp<XOR1>(ml);
        const float ms = (l & 1) ? mo : ml, ls = (l & 1) ? ml : mo;
        const float M = max16(ms);
        const float e = __builtin_amdgcn_exp2f((ms - M) * LOG2E);
        const float f = e * __builtin_amdgcn_rcpf(0.5f * sum16(e * ls));
#pragma unroll
        for (int i = 0; i < 8; ++i) {
          const float fa = __builtin_bit_cast(float, __builtin_amdgcn_readlane(__builtin_bit_cast(int, f), 2 * i));
          const float fb = __builtin_bit_cast(float, __builtin_amdgcn_readlane(__builtin_bit_cast(int, f), 16 + 2 * i));
          fi[i] = hh == ha ? fa : fb;
          asm volatile("" : "+v"(fi[i]));
        }
      }
      if (GDELAY) __builtin_amdgcn_s_sleep(GDELAY);
      float o0, o1;
      for (unsigned spins = 0;; ++spins) {
#pragma unroll
        for (int i = 0; i < 8; ++i) g[i] = get_gran(og + i * 128);
        SB();
        unsigned bad = 0;
        o0 = 0.f; o1 = 0.f;
#pragma unroll
        for (int i = 0; i < 8; ++i) {
          const unsigned v = (unsigned)(g[i] >> 32);
          bad |= (unsigned)g[i] ^ tlo;
          o0 = fmaf(fi[i], bf2f(v & 0xffffu), o0);
          o1 = fmaf(fi[i], __uint_as_float(v & 0xffff0000u), o1);
          asm volatile("" : "+v"(o0), "+v"(o1), "+v"(bad));
        }
        if (__all(bad == 0) || spins > SPIN_CAP) break;
        if (GSLEEP) __builtin_amdgcn_s_sleep(GSLEEP);
      }
      *odst = v2u{cvtpk(o0, o0), cvtpk(o1, o1)};
    } else if (w < 8) {
      const f32x4 z = mfma(mk & ((LAS unsigned*)(lds + L_HB))[8 * w + (l >> 3)], W2, zero4);
      if (valid) *(LAS f32x4*)(lds + L_ZP + (2 * w + (l >> 5)) * YP_STR + dcol) = z;
      lds_signal(flags + 5, l);
      if (w == 0) {
        lds_wait_ge(flags + 5, 8);
        LAS float* src = lds + L_ZP + ypg * YP_STR + 4 * yc4;
        f32x4 t = (*(LAS f32x4*)(src) + *(LAS f32x4*)(src + 4 * YP_STR)) + (*(LAS f32x4*)(src + 8 * YP_STR) + *(LAS f32x4*)(src + 12 * YP_STR));
#pragma unroll
        for (int r = 0; r < 4; ++r) { t[r] += dpp<XOR1>(t[r]); t[r] += dpp<XOR2>(t[r]); }
        zb = cvtpk(0.f, (ypg & 2) ? ((ypg & 1) ? t[3] : t[2]) : ((ypg & 1) ? t[1] : t[0]));
        asm volatile("" : "+v"(zb));
      }
    }
    if (DIAGACK && w > 0 && w < 14) { VMWAIT(0); TS(2); }
    TS(9);
    BAR();
    {
      const unsigned o0 = osrc[0], o1 = osrc[8];
      f32x4 y = mfma(mk & o0, WO[0], zero4);
      y = mfma(mk & o1, WO[1], y);
      if (valid) *(LAS f32x4*)ydst = y;
    }
    BAR();
    if (w == 0) {
      f32x4 t[8];
#pragma unroll
      for (int u = 0; u < 8; ++u) t[u] = *(LAS f32x4*)(ysrc + 4 * u * YP_STR);
      f32x4 a = ((t[0] + t[1]) + (t[2] + t[3])) + ((t[4] + t[5]) + (t[6] + t[7]));
#pragma unroll
      for (int r = 0; r < 4; ++r) { a[r] += dpp<XOR1>(a[r]); a[r] += dpp<XOR2>(a[r]); }
      const float ys = (ypg & 2) ? ((ypg & 1) ? a[3] : a[2]) : ((ypg & 1) ? a[1] : a[0]);
      put_gran2(ydg, (cvtpk(ys, ys) & 0xffffu) | zb, tlo);
      TS(10);
      if (DIAGACK) { VMWAIT(0); TS(3); }
    }
  }
  if (fin) {
    float sy, sz;
    if (FDELAY) __builtin_amdgcn_s_sleep(FDELAY);
    for (unsigned spins = 0;; ++spins) {
      u64 g[6];
#pragma unroll
      for (int u = 0; u < 6; ++u) g[u] = get_gran(ys + u * 4 * 64);
      SB();
      unsigned bad = 0;
      sy = 0.f; sz = 0.f;
#pragma unroll
      for (int u = 0; u < 6; ++u) {
        const unsigned v = (unsigned)(g[u] >> 32);
        bad |= (unsigned)g[u] ^ tlo;
        sy += bf2f(v & 0xffffu); sz += __uint_as_float(v & 0xffff0000u);
        asm volatile("" : "+v"(sy), "+v"(sz), "+v"(bad));
      }
      if (__all(bad == 0) || spins > SPIN_CAP) break;
      if (FSLEEP) __builtin_amdgcn_s_sleep(FSLEEP);
    }
    sy = xrow_sum(sy); sz = xrow_sum(sz);
    const float bb = (l & 1) ? __uint_as_float(b2fin & 0xffff0000u) : bf2f(b2fin & 0xffffu);
    const float xx = (l & 1) ? __uint_as_float(xfin & 0xffff0000u) : bf2f(xfin & 0xffffu);
    const float o2 = rbf(rbf(sz) + bb);
    const float gt = rbf(o2 * __builtin_amdgcn_rcpf(1.0f + __builtin_amdgcn_exp2f(-o2 * LOG2E)));
    const float res = xx + rbf(rbf(sy) * gt);
    const unsigned pk = cvtpk(res, dpp<XOR1>(res));
    TS(11);
    while (__builtin_amdgcn_s_memrealtime() < tpr + 4000) __builtin_amdgcn_s_sleep(1);
    if (l < 16 && !(l & 1)) ((GAS unsigned*)p.out)[8 * fq + (l >> 1)] = pk;
    if (fb == 0 && l == 0) __hip_atomic_store(ws + G_EPOCH, tag, RLX_AGENT);
    TS(11);
  }
}

static inline size_t ws_offset(size_t) { return 0; }

extern "C" void kernel_launch(void* const* d_in, const int* in_sizes, int n_in,
                              void* d_out, int out_size, void* d_ws, size_t ws_size,
                              hipStream_t stream) {
  Params p;
  p.x = (const unsigned short*)d_in[0];
  p.k = (const unsigned short*)d_in[1];
  p.v = (const unsigned short*)d_in[2];
  p.wq = (const unsigned short*)d_in[3];
  p.wo = (const unsigned short*)d_in[4];
  p.w1 = (const unsigned short*)d_in[6];
  p.b1 = (const unsigned short*)d_in[7];
  p.w2 = (const unsigned short*)d_in[8];
  p.b2 = (const unsigned short*)d_in[9];
  p.out = (unsigned short*)d_out;
  p.ws = (u64*)((char*)d_ws + ws_offset(ws_size));
  hipLaunchKernelGGL(fused_decode, dim3(NWG), dim3(NT), 0, stream, p);
}
```

```cpp
#include <hip/hip_runtime.h>
#include <hip/hip_bf16.h>
#include <stdint.h>

typedef unsigned v4u __attribute__((ext_vector_type(4)));
typedef unsigned v2u __attribute__((ext_vector_type(2)));
typedef float f32x4 __attribute__((ext_vector_type(4)));
typedef short bf16x8 __attribute__((ext_vector_type(8)));
typedef __bf16 bf16x2_t __attribute__((ext_vector_type(2)));
typedef unsigned long long u64;
#define GAS __attribute__((address_space(1)))
#define LAS __attribute__((address_space(3)))
#define RLX_AGENT __ATOMIC_RELAXED, __HIP_MEMORY_SCOPE_AGENT
#define RLX_WG __ATOMIC_RELAXED, __HIP_MEMORY_SCOPE_WORKGROUP
typedef GAS u64 gu64;

#define WIN 2
#define WIN0 2
#define QI 4
#define GI 1
#define EI 9
#define WINE 4
#define PUBPOS 5
#define WOPOS 8
#define WOSPLIT 1
#define WOIMM 0
#define GDELAY 0
#define FB0 16
#define GSLEEP 0
#define FSLEEP 0
#define FDELAY 0
#define F128 0
#define MLPRE 1
#define PACEA 900
#define PACEX 0
#define PACEB 620
#define PT 0
#define WOPACE 1
#define WOT0 600
#define WOT1 1200
#define TLBT 0
#define TLBN 8
#define FW16 1
#define FSTR 5
#define PRET 0
#define XMAP 1
#define ANCHOR 1
#define MK 8
#define MG 0
#define PACEC 0
#define PACE0 0
#define WOGATE 1
#define DIAGACK 0
#define K_AUX 2
#define V_AUX 2
#define W_AUX 2

constexpr int DM = 640, HQ = 32, DH = 192, SK = 32768, WN = 4096, FF = 1536;
constexpr int NWG = 256, NT = 1024;
constexpr int ROWB = DH * 2;
constexpr int GRPB = 8 * ROWB;
constexpr float LOG2E = 1.4426950408889634f;
constexpr int RSRC_W3 = 0x00020000;
constexpr unsigned SPIN_CAP = 1u << 18;

constexpr size_t G_EPOCH = 0;
constexpr size_t G_T = 64;
constexpr size_t G_Q = G_T + 256;
constexpr size_t G_O = G_Q + 256 * 64;
constexpr size_t G_Y = G_O + (size_t)NWG * 128;
constexpr size_t G_END = G_Y + (size_t)240 * 64;
constexpr size_t G_TS = G_END + 64;

constexpr int QP_STR = 68;
constexpr int OP_STR = 200;
constexpr int YP_STR = 80;
constexpr int L_QP = 0;
constexpr int L_OP = L_QP + 20 * QP_STR;
constexpr int L_YP = L_OP + 32 * OP_STR;
constexpr int L_ZP = L_YP + 32 * YP_STR;
constexpr int L_X = L_ZP + 16 * YP_STR;
constexpr int L_QB = L_X + 512;
constexpr int L_OB = L_QB + 96;
constexpr int L_HB = L_OB + 256;
constexpr int L_M = L_HB + 64;
constexpr int L_L = L_M + 16;
constexpr int L_T = L_L + 16;
constexpr int L_FLAG = L_T + 4;
constexpr int L_END = L_FLAG + 8;

struct Params {
  const unsigned short* x;
  const unsigned short* k;
  const unsigned short* v;
  const unsigned short* wq;
  const unsigned short* wo;
  const unsigned short* w1;
  const unsigned short* b1;
  const unsigned short* w2;
  const unsigned short* b2;
  unsigned short* out;
  u64* ws;
};

__device__ __forceinline__ float bf2f(unsigned h) { return __builtin_bit_cast(float, h << 16); }
__device__ __forceinline__ unsigned cvtpk(float lo, float hi) {
  bf16x2_t r = {(__bf16)lo, (__bf16)hi};
  return __builtin_bit_cast(unsigned, r);
}
__device__ __forceinline__ float rbf(float f) { return bf2f(cvtpk(f, f) & 0xffffu); }
__device__ __forceinline__ float dot2(unsigned a, unsigned b, float acc) {
  return __builtin_amdgcn_fdot2_f32_bf16(__builtin_bit_cast(bf16x2_t, a), __builtin_bit_cast(bf16x2_t, b), acc, false);
}
template <int CTRL> __device__ __forceinline__ float dpp(float x) {
  return __builtin_bit_cast(float, __builtin_amdgcn_mov_dpp(__builtin_bit_cast(int, x), CTRL, 0xf, 0xf, true));
}
constexpr int XOR1 = 0xB1, XOR2 = 0x4E, HMIR = 0x141, ROR8 = 0x128;
__device__ __forceinline__ float sum8(float x) { x += dpp<XOR1>(x); x += dpp<XOR2>(x); x += dpp<HMIR>(x); return x; }
__device__ __forceinline__ float sum16(float x) { x = sum8(x); x += dpp<ROR8>(x); return x; }
__device__ __forceinline__ float max16(float x) {
  x = fmaxf(x, dpp<XOR1>(x)); x = fmaxf(x, dpp<XOR2>(x)); x = fmaxf(x, dpp<HMIR>(x)); x = fmaxf(x, dpp<ROR8>(x)); return x; }
__device__ __forceinline__ float xrow_sum(float x) {
  auto s = __builtin_amdgcn_permlane16_swap(__float_as_uint(x), __float_as_uint(x), false, false);
  x = __uint_as_float(s[0]) + __uint_as_float(s[1]);
  auto t = __builtin_amdgcn_permlane32_swap(__float_as_uint(x), __float_as_uint(x), false, false);
  return __uint_as_float(t[0]) + __uint_as_float(t[1]);
}
__device__ __forceinline__ float xrow_max(float x) {
  auto s = __builtin_amdgcn_permlane16_swap(__float_as_uint(x), __float_as_uint(x), false, false);
  x = fmaxf(__uint_as_float(s[0]), __uint_as_float(s[1]));
  auto t = __builtin_amdgcn_permlane32_swap(__float_as_uint(x), __float_as_uint(x), false, false);
  return fmaxf(__uint_as_float(t[0]), __uint_as_float(t[1]));
}
__device__ __forceinline__ f32x4 mfma(v4u a, v4u b, f32x4 c) {
  return __builtin_amdgcn_mfma_f32_16x16x32_bf16(__builtin_bit_cast(bf16x8, a), __builtin_bit_cast(bf16x8, b), c, 0, 0, 0);
}
__device__ __forceinline__ __amdgpu_buffer_rsrc_t mkrsrc(const void* p, unsigned bytes) {
  return __builtin_amdgcn_make_buffer_rsrc((void*)p, 0, bytes, RSRC_W3);
}
#define SB() __builtin_amdgcn_sched_barrier(0)
#define VMWAIT(n) asm volatile("s_waitcnt vmcnt(%0)" ::"n"(n) : "memory")
#define LGKM0() asm volatile("s_waitcnt lgkmcnt(0)" ::: "memory")
#define BAR() do { LGKM0(); __builtin_amdgcn_s_barrier(); asm volatile("" ::: "memory"); } while (0)

__device__ __forceinline__ void put_gran(gu64* g, float v, unsigned tlo, unsigned thi) {
  const unsigned hi = (__float_as_uint(v) & 0xffffff00u) | thi;
  __hip_atomic_store(g, ((u64)hi << 32) | tlo, RLX_AGENT);
}
__device__ __forceinline__ bool gran_ok(u64 g, unsigned tlo, unsigned thi) {
  return ((unsigned)g == tlo) & (((unsigned)(g >> 32) & 0xffu) == thi);
}
__device__ __forceinline__ float gran_val(u64 g) { return __uint_as_float((unsigned)(g >> 32) & 0xffffff00u); }
__device__ __forceinline__ void put_gran2(gu64* g, unsigned v, unsigned tlo) { __hip_atomic_store(g, ((u64)v << 32) | tlo, RLX_AGENT); }
__device__ __forceinline__ u64 get_gran(gu64* g) { return __hip_atomic_load(g, RLX_AGENT); }

__device__ __forceinline__ void lds_wait_ge(LAS unsigned* f, unsigned n) {
  while (__hip_atomic_load(f, RLX_WG) < n) __builtin_amdgcn_s_sleep(1);
  asm volatile("" ::: "memory");
}
__device__ __forceinline__ int opaque0() { int z; asm("v_mov_b32 %0, 0" : "=v"(z)); return z; }
__device__ __forceinline__ void lds_signal(LAS unsigned* f, int lane) {
  LGKM0();
  if (lane == 0) (void)__hip_atomic_fetch_add(f + opaque0(), 1u, RLX_WG);
}
__device__ __forceinline__ float qk_score(v4u k0, v4u k1, v4u k2, const v4u (&qf)[3]) {
  float d0 = dot2(k0.x, qf[0].x, 0.f), d1 = dot2(k0.y, qf[0].y, 0.f);
  d0 = dot2(k0.z, qf[0].z, d0); d1 = dot2(k0.w, qf[0].w, d1);
  d0 = dot2(k1.x, qf[1].x, d0); d1 = dot2(k1.y, qf[1].y, d1);
  d0 = dot2(k1.z, qf[1].z, d0); d1 = dot2(k1.w, qf[1].w, d1);
  d0 = dot2(k2.x, qf[2].x, d0); d1 = dot2(k2.y, qf[2].y, d1);
  d0 = dot2(k2.z, qf[2].z, d0); d1 = dot2(k2.w, qf[2].w, d1);
  return rbf(rbf(sum8(d0 + d1)) * 0.07216878364870322f);
}
#define PACET(t) do { while ((int)((unsigned)__builtin_amdgcn_s_memrealtime() - tref) < (t) / 10) __builtin_amdgcn_s_sleep(1); } while (0)
#define PACE(j) do { if ((j) >= 12 + MG + 3) PACET(PACEC + PACEB * ((j) - MK)); else if (PACE0) PACET(PACEA + PACE0 * (j)); } while (0)
#define TWO(t) (PACEC + PACEB * (23 - MK) + (t))
#define KLOAD(i) __builtin_amdgcn_raw_buffer_load_b128(rk, voff + ((i) % 3) * 128, woff + ((i) / 3) * GRPB, K_AUX)
#define VLOAD(i) __builtin_amdgcn_raw_buffer_load_b128(rv, voff + ((i) % 3) * 128, woff + ((i) / 3) * GRPB, V_AUX)

#define TS(i) do {} while (0)

__global__ void __launch_bounds__(NT) fused_decode(Params p) {
  __shared__ __attribute__((aligned(16))) float lds_[L_END];
  LAS float* lds = (LAS float*)lds_;
  LAS unsigned* flags = (LAS unsigned*)(lds + L_FLAG);
  const int tid = threadIdx.x, l = tid & 63, w = __builtin_amdgcn_readfirstlane(tid >> 6);
  const int b = blockIdx.x;
  const int s = XMAP ? b & 7 : (b >> 3) & 7, h = XMAP ? b >> 3 : (b & 7) * 4 + (b >> 6);
  const bool outp = b >= 16;
  const int rb = outp ? (b - 16) / 10 : 0, cb = outp ? (b - 16) % 10 : 0;
  TS(0);
  gu64* ws = (gu64*)p.ws;
  unsigned tref = (unsigned)__builtin_amdgcn_s_memrealtime();

  const unsigned voff = (unsigned)((l >> 3) * ROWB + (l & 7) * 16);
  const size_t kvoff = ((size_t)h * SK + (SK - WN) + (size_t)s * (WN / 8)) * DH;
  const __amdgpu_buffer_rsrc_t rk = mkrsrc(p.k + kvoff, (WN / 8) * ROWB);
  const __amdgpu_buffer_rsrc_t rv = mkrsrc(p.v + kvoff, (WN / 8) * ROWB);
  const int woff = w * (4 * GRPB);

  const bool proj = w < 10 && b < 240;
  const int row0 = (b & 1) * 320 + 32 * w;
  v4u K[12], V[12], A[4], X0, X1;
  if (__builtin_expect(proj, 1)) {
    const int ld = b < 192 ? HQ * DH : FF;
    const unsigned short* wb = b < 192 ? p.wq + (b >> 1) * 64 : p.w1 + ((b - 192) >> 1) * 64;
    const __amdgpu_buffer_rsrc_t ra = mkrsrc(wb + (size_t)row0 * ld, 32 * ld * 2);
    const __amdgpu_buffer_rsrc_t rx = mkrsrc(p.x, DM * 2);
    const unsigned aoff = (unsigned)((l >> 3) * ld * 2 + (l & 7) * 16);
    X0 = __builtin_amdgcn_raw_buffer_load_b128(rx, l * 16, 0, 0);
    X1 = __builtin_amdgcn_raw_buffer_load_b128(rx, l * 16 + 1024, 0, 0);
#pragma unroll
    for (int i = 0; i < 4; ++i) A[i] = __builtin_amdgcn_raw_buffer_load_b128(ra, aoff, i * 8 * ld * 2, W_AUX);
  }
  SB();
#pragma unroll
  for (int i = 0; i < WIN0; ++i) K[i] = KLOAD(i);
  SB();
  unsigned touch = 0;
  if (PRET && w == 15) {
    const char* a = (const char*)(p.v + kvoff);
    a = l == 1 ? (const char*)(p.v + kvoff) + (WN / 8) * ROWB - 4 : a;
    a = l == 2 ? (const char*)(p.k + kvoff) + (WN / 8) * ROWB - 4 : a;
    a = l == 3 ? (const char*)(p.wo + (size_t)(256 * rb) * DM + 64 * cb) : a;
    a = l == 4 ? (const char*)(p.wo + (size_t)(256 * rb + 255) * DM + 64 * cb) : a;
    a = l == 5 ? (const char*)(p.w2 + (size_t)(64 * rb) * DM + 64 * cb) : a;
    a = l == 6 ? (const char*)(p.w2 + (size_t)(64 * rb + 63) * DM + 64 * cb) : a;
    a = l == 7 ? (const char*)p.out : a;
    a = l == 8 ? (const char*)p.b1 : a;
    a = l == 9 ? (const char*)p.b2 : a;
    a = l == 10 ? (const char*)(p.ws + G_END) : a;
    if (l < 11) touch = __builtin_nontemporal_load((const GAS unsigned*)a);
    SB();
  }
  if (tid < 8) flags[tid] = 0;
  TS(12);
  BAR();
  TS(13);
  const u64 epv = get_gran(ws + G_EPOCH);
  SB();

  v4u mk;
  {
    const unsigned hm = (l & 1) ? 0xffff0000u : 0x0000ffffu;
    const int q4 = (l & 7) >> 1;
    mk.x = q4 == 0 ? hm : 0u; mk.y = q4 == 1 ? hm : 0u; mk.z = q4 == 2 ? hm : 0u; mk.w = q4 == 3 ? hm : 0u;
  }
  const bool valid = ((l >> 3) & 1) == (l >> 5);
  const int dcol = 8 * (l & 7) + 4 * ((l >> 4) & 1);
  const f32x4 zero4 = {0.f, 0.f, 0.f, 0.f};

  if (proj) {
    *(LAS v4u*)(lds + L_X + 4 * l) = X0;
    *(LAS v4u*)(lds + L_X + 256 + 4 * l) = X1;
    unsigned xr[4];
#pragma unroll
    for (int i = 0; i < 4; ++i) xr[i] = ((LAS unsigned short*)(lds + L_X))[row0 + 8 * i + (l >> 3)];
    float xs[16], sum = 0.f;
#pragma unroll
    for (int i = 0; i < 4; ++i) {
      xs[2 * i] = bf2f(X0[i] & 0xffffu); xs[2 * i + 1] = __uint_as_float(X0[i] & 0xffff0000u);
      xs[8 + 2 * i] = bf2f(X1[i] & 0xffffu); xs[8 + 2 * i + 1] = __uint_as_float(X1[i] & 0xffff0000u);
    }
#pragma unroll
    for (int i = 0; i < 8; ++i) sum += xs[i] + xs[8 + i];
    sum = xrow_sum(sum16(sum));
    const float mean = sum * (1.0f / DM);
    float sq = 0.f, sq1 = 0.f;
#pragma unroll
    for (int i = 0; i < 8; ++i) { const float d = xs[i] - mean, e = xs[8 + i] - mean; sq = fmaf(d, d, sq); sq1 = fmaf(e, e, sq1); }
    sq += l < 16 ? sq1 : 0.f;
    sq = xrow_sum(sum16(sq));
    const float sd = rbf(sqrtf(rbf(rbf(sq * (1.0f / DM)) + 1e-5f)));
    const float mb = rbf(mean);
    f32x4 d = zero4;
#pragma unroll
    for (int i = 0; i < 4; ++i) {
      const float xn = rbf(bf2f(xr[i]) - mb) / sd;
      d = mfma(mk & cvtpk(xn, xn), A[i], d);
    }
    if (valid) *(LAS f32x4*)(lds + L_QP + (2 * w + (l >> 5)) * QP_STR + dcol) = d;
    lds_signal(flags + 0, l);
    TS(1);
  }

  u64 tag;
#pragma unroll
  for (int i = WIN0; i < 12; ++i) {
    if ((i == PUBPOS + 1 || i == PUBPOS + 2) && w == 10) VMWAIT(WIN);
    else VMWAIT(WIN - 1);
    PACE(i);
    K[i] = KLOAD(i);
    SB();
    if (i == 3) TS(14);
    if (ANCHOR && i == MK && w == 11 && l == 0) put_gran2(ws + G_T + b, (unsigned)__builtin_amdgcn_s_memrealtime(), (unsigned)tag);
    if (i == PUBPOS) {
      if (PRET) asm volatile("" ::"v"(touch));
      tag = ((u64)__builtin_amdgcn_readfirstlane((unsigned)(epv >> 32)) << 32 | (u64)__builtin_amdgcn_readfirstlane((unsigned)epv)) + 1;
      if (w == 10 && b < 240) {
        lds_wait_ge(flags + 0, 10);
        float acc = 0.f;
#pragma unroll
        for (int u = 0; u < 20; ++u) acc += lds[L_QP + u * QP_STR + l];
        put_gran(ws + G_Q + b * 64 + l, acc, (unsigned)tag, (unsigned)(tag >> 32) & 0xffu);
        TS(2);
      }
      SB();
    }
  }
  const unsigned tlo = (unsigned)tag, thi = (unsigned)(tag >> 32) & 0xffu;

  u64 g0 = 0, g1 = 0;
  unsigned b1v = 0;
  gu64* qsrc = ws + G_Q + (w < 15 ? 3 * h + (w - 12) : 96 + rb) * 128 + l;
  if (w >= 12) {
    g0 = get_gran(qsrc); g1 = get_gran(qsrc + 64);
    b1v = ((const GAS unsigned*)p.b1)[rb * 32 + (l >> 1)];
  }
  SB();

  float sc[4], mwg;
  v4u qf[3], sel[4], WO[2], W2;
  f32x4 acc[3] = {zero4, zero4, zero4};
  const unsigned oo = (unsigned)((l >> 3) * (DM * 2) + (l & 7) * 16);
  const __amdgpu_buffer_rsrc_t ro = mkrsrc(p.wo + (size_t)(256 * rb + 16 * w) * DM + 64 * cb, outp ? 16 * DM * 2 : 0);
  auto load_wo = [&](int lo, int hi, bool paced) {
#pragma unroll
    for (int u = lo; u < hi; ++u) {
      if (paced) VMWAIT(WIN - 1);
      if (u == 0) {
        const __amdgpu_buffer_rsrc_t r2 = mkrsrc(p.w2 + (size_t)(64 * rb + 8 * (w & 7)) * DM + 64 * cb, outp && w < 8 ? 8 * DM * 2 : 0);
        W2 = __builtin_amdgcn_raw_buffer_load_b128(r2, oo, 0, W_AUX);
      } else {
        WO[u - 1] = __builtin_amdgcn_raw_buffer_load_b128(ro, oo, (u - 1) * 8 * DM * 2, W_AUX);
      }
      SB();
    }
  };
  const int ha = (128 * rb) / 96;
  gu64* mlp = ws + G_O + (size_t)((ha + ((l >> 4) & 1)) * 8 + ((l >> 1) & 7)) * 128 + 96 + (l & 1);
  u64 gm = 0, gt = 0;
  const int d = (w & 1) * 64 + l, pr = 128 * rb + d;
  const int hh = pr / 96;
  gu64* og = ws + G_O + (size_t)(hh * 8) * 128 + (pr - hh * 96);
  const int fb = b - FB0;
  const bool fin = w == 10 && fb >= 0 && fb < 40 * FSTR && fb % FSTR == 0;
  const int fq = fb / FSTR;
  gu64* ys = ws + G_Y + (size_t)(((fq >> 2) & 15) * 24 + (l >> 4)) * 64 + 16 * (fq & 3) + (l & 15);
  unsigned tlbv = 0;
  unsigned ptv[8] = {0, 0, 0, 0, 0, 0, 0, 0};
#pragma unroll
  for (int i = 0; i < 12; ++i) {
    if (PT && (i == PT + 1 || i == PT + 2) && w >= 14) VMWAIT(WIN + 7);
    else if (PT && (i == PT + 1 || i == PT + 2) && w == 10) VMWAIT(WIN);
    else if (i >= EI) VMWAIT(WINE - 1);
    else if (i < WIN && w >= 12) VMWAIT(WIN + 2);
    else VMWAIT(WIN - 1);
    if (ANCHOR && i == MG + 3) {
      lds_wait_ge(flags + 6, 1);
      tref = __builtin_amdgcn_readfirstlane(((LAS unsigned*)lds)[L_T]);
    }
    PACE(12 + i);
    V[i] = VLOAD(i);
    SB();
    if (MLPRE && i == 11 && w >= 14) gm = get_gran(mlp);
    if (ANCHOR && i == MG && w == 9) gt = get_gran(ws + G_T + (b & ~63) + l);
    if (ANCHOR && i == MG + 2 && w == 9) {
      const bool ok = (unsigned)gt == tlo;
      const int n = __builtin_popcountll(__builtin_amdgcn_ballot_w64(ok));
      const float dt = xrow_sum(sum16(ok ? (float)(int)((unsigned)(gt >> 32) - tref) : 0.f));
      if (l == 0) ((LAS unsigned*)lds)[L_T] = n >= 16 ? tref + (unsigned)(int)(dt * __builtin_amdgcn_rcpf((float)n)) : tref - 0x1000000u;
      lds_signal(flags + 6, l);
    }

    if (PT && i == PT) {
      if (w >= 14) {
#pragma unroll
        for (int u = 0; u < 8; ++u) ptv[u] = __hip_atomic_load((GAS unsigned*)(og + u * 128), RLX_AGENT);
      } else if (fin) {
        ptv[0] = __hip_atomic_load((GAS unsigned*)ys, RLX_AGENT);
      }
      SB();
    }
    if (TLBT && i == TLBT && w == 9 && outp) {
      if (l < TLBN) tlbv = __builtin_nontemporal_load((const GAS unsigned*)(p.wo + (size_t)(256 * rb + (256 / TLBN) * l) * DM + 64 * cb));
      SB();
    }
    if (TLBT && i == TLBT + 3) asm volatile("" ::"v"(tlbv));
    if (PT && i == PT + 3) asm volatile("" ::"v"(ptv[0]), "v"(ptv[1]), "v"(ptv[2]), "v"(ptv[3]), "v"(ptv[4]), "v"(ptv[5]), "v"(ptv[6]), "v"(ptv[7]));
    if (i == GI && w >= 12) {
      if (__builtin_expect(!__all(gran_ok(g0, tlo, thi) & gran_ok(g1, tlo, thi)), 0)) {
#pragma clang loop unroll(disable)
        for (unsigned spins = 0; spins < SPIN_CAP; ++spins) {
          __builtin_amdgcn_s_sleep(4);
          g0 = get_gran(qsrc); g1 = get_gran(qsrc + 64);
          if (__all(gran_ok(g0, tlo, thi) & gran_ok(g1, tlo, thi))) break;
        }
      }
      const float tot = gran_val(g0) + gran_val(g1);
      if (w < 15) {
        ((LAS unsigned short*)(lds + L_QB))[(w - 12) * 64 + l] = (unsigned short)(cvtpk(tot, tot) & 0xffffu);
      } else {
        float hv = rbf(rbf(tot) + ((l & 1) ? __uint_as_float(b1v & 0xffff0000u) : bf2f(b1v & 0xffffu)));
        hv = hv > 0.f ? hv : 0.f;
        hv = hv * hv;
        ((LAS unsigned*)(lds + L_HB))[l] = cvtpk(hv, hv);
      }
      lds_signal(flags + 1, l);
      TS(3);
    }
    if (i == QI) {
      TS(4);
      lds_wait_ge(flags + 1, 4);
#pragma unroll
      for (int j = 0; j < 3; ++j) qf[j] = *(LAS v4u*)(lds + L_QB + 32 * j + 4 * (l & 7));
      TS(5);
      sc[0] = qk_score(K[0], K[1], K[2], qf);
      sc[1] = qk_score(K[3], K[4], K[5], qf);
    }
    if (i == QI + 1) {
      sc[2] = qk_score(K[6], K[7], K[8], qf);
      sc[3] = qk_score(K[9], K[10], K[11], qf);
      float mw = fmaxf(fmaxf(sc[0], sc[1]), fmaxf(sc[2], sc[3]));
      mw = xrow_max(fmaxf(mw, dpp<ROR8>(mw)));
      if (l == 0) lds[L_M + w] = mw;
      lds_signal(flags + 2, l);
    }
    if (i == QI + 3) {
      lds_wait_ge(flags + 2, 16);
      mwg = max16(lds[L_M + (l & 15)]);
      float lw = 0.f;
#pragma unroll
      for (int g = 0; g < 4; ++g) {
        const float pr = __builtin_amdgcn_exp2f((sc[g] - mwg) * LOG2E);
        lw += pr;
        sel[g] = mk & cvtpk(pr, pr);
      }
      lw = xrow_sum(lw + dpp<ROR8>(lw));
      if (l == 0) lds[L_L + w] = lw;
      lds_signal(flags + 4, l);
#pragma unroll
      for (int u = 0; u < 6; ++u) acc[u % 3] = mfma(sel[u / 3], V[u], acc[u % 3]);
      TS(6);
    }
    if (i == 10) {
#pragma unroll
      for (int u = 6; u < 9; ++u) acc[u % 3] = mfma(sel[2], V[u], acc[u % 3]);
    }
    SB();
    if (i == WOPOS) load_wo(0, WOSPLIT, true);
    if (i == QI + 5 && w == 11) {
      lds_wait_ge(flags + 4, 16);
      const float lsum = sum16(lds[L_L + (l & 15)]);
      if (l < 16) put_gran(ws + G_O + (size_t)(h * 8 + s) * 128 + 96 + l, l == 0 ? mwg : lsum, tlo, thi);
      SB();
    }
  }
  if (WOPACE) {
    PACET(TWO(WOT0));
    if (WOGATE) { lds_signal(flags + 3, l); lds_wait_ge(flags + 3, 16); }
    load_wo(WOSPLIT, WOPACE == 1 ? 2 : 3, false);
  } else {
    lds_signal(flags + 3, l);
    if (WOIMM) {
      lds_wait_ge(flags + 3, WOIMM);
      load_wo(WOSPLIT, 3, false);
    }
  }
#pragma unroll
  for (int u = 9; u < 12; ++u) acc[u % 3] = mfma(sel[3], V[u], acc[u % 3]);
  {
    LAS float* op = lds + L_OP + (w + 16 * (l >> 5)) * OP_STR + dcol;
#pragma unroll
    for (int j = 0; j < 3; ++j) if (valid) *(LAS f32x4*)(op + 64 * j) = acc[j];
  }
  TS(7);
  if (!WOIMM && !WOPACE) {
    lds_wait_ge(flags + 3, 16);
    load_wo(WOSPLIT, 3, false);
  }
  TS(15);
  BAR();
  if (w < 6) {
    const int c4 = tid >> 3, pg = tid & 7;
    LAS float* src = lds + L_OP + pg * OP_STR + 4 * c4;
    f32x4 t = (*(LAS f32x4*)(src) + *(LAS f32x4*)(src + 8 * OP_STR)) + (*(LAS f32x4*)(src + 16 * OP_STR) + *(LAS f32x4*)(src + 24 * OP_STR));
#pragma unroll
    for (int r = 0; r < 4; ++r) t[r] = sum8(t[r]);
    const unsigned pk = (pg & 1) ? cvtpk(t[2], t[3]) : cvtpk(t[0], t[1]);
    if (pg < 2) put_gran2(ws + G_O + (size_t)(h * 8 + s) * 128 + 2 * c4 + pg, pk, tlo);
  }
  TS(8);
  if (WOPACE == 1) {
    PACET(TWO(WOT1));
    load_wo(2, 3, false);
  }
  unsigned xfin = 0, b2fin = 0;
  if (fin) {
    xfin = ((const GAS unsigned*)p.x)[8 * fq + ((l & 15) >> 1)];
    b2fin = ((const GAS unsigned*)p.b2)[8 * fq + ((l & 15) >> 1)];
  }
  asm volatile("" : "+v"(ys));
  const int yc4 = l >> 2, ypg = l & 3;
  unsigned zb = 0;
  if (outp) {
    LAS unsigned* osrc = (LAS unsigned*)(lds + L_OB) + 16 * w + (l >> 3);
    LAS float* ydst = lds + L_YP + (2 * w + (l >> 5)) * YP_STR + dcol;
    asm volatile("" : "+v"(osrc), "+v"(ydst));
    LAS float* ysrc = lds + L_YP + ypg * YP_STR + 4 * yc4;
    gu64* ydg = ws + G_Y + (size_t)(cb * 24 + rb) * 64 + l;
    asm volatile("" : "+v"(ysrc), "+v"(ydg));
    if (w >= 14) {
      LAS v2u* odst = (LAS v2u*)(lds + L_OB + 2 * d);
      asm volatile("" : "+v"(odst));
      u64 g[8];
      float fi[8];
      if (!__all(gran_ok(gm, tlo, thi))) {
#pragma clang loop unroll(disable)
        for (unsigned spins = 0; spins < SPIN_CAP; ++spins) {
          gm = get_gran(mlp);
          if (__all(gran_ok(gm, tlo, thi))) break;
          __builtin_amdgcn_s_sleep(2);
        }
      }
      {
        const float ml = gran_val(gm);
        const float mo = dpp<XOR1>(ml);
        const float ms = (l & 1) ? mo : ml, ls = (l & 1) ? ml : mo;
        const float M = max16(ms);
        const float e = __builtin_amdgcn_exp2f((ms - M) * LOG2E);
        const float f = e * __builtin_amdgcn_rcpf(0.5f * sum16(e * ls));
#pragma unroll
        for (int i = 0; i < 8; ++i) {
          const float fa = __builtin_bit_cast(float, __builtin_amdgcn_readlane(__builtin_bit_cast(int, f), 2 * i));
          const float fb = __builtin_bit_cast(float, __builtin_amdgcn_readlane(__builtin_bit_cast(int, f), 16 + 2 * i));
          fi[i] = hh == ha ? fa : fb;
          asm volatile("" : "+v"(fi[i]));
        }
      }
      if (GDELAY) __builtin_amdgcn_s_sleep(GDELAY);
      float o0, o1;
      for (unsigned spins = 0;; ++spins) {
#pragma unroll
        for (int i = 0; i < 8; ++i) g[i] = get_gran(og + i * 128);
        SB();
        unsigned bad = 0;
        o0 = 0.f; o1 = 0.f;
#pragma unroll
        for (int i = 0; i < 8; ++i) {
          const unsigned v = (unsigned)(g[i] >> 32);
          bad |= (unsigned)g[i] ^ tlo;
          o0 = fmaf(fi[i], bf2f(v & 0xffffu), o0);
          o1 = fmaf(fi[i], __uint_as_float(v & 0xffff0000u), o1);
          asm volatile("" : "+v"(o0), "+v"(o1), "+v"(bad));
        }
        if (__all(bad == 0) || spins > SPIN_CAP) break;
        if (GSLEEP) __builtin_amdgcn_s_sleep(GSLEEP);
      }
      *odst = v2u{cvtpk(o0, o0), cvtpk(o1, o1)};
    } else if (w < 8) {
      const f32x4 z = mfma(mk & ((LAS unsigned*)(lds + L_HB))[8 * w + (l >> 3)], W2, zero4);
      if (valid) *(LAS f32x4*)(lds + L_ZP + (2 * w + (l >> 5)) * YP_STR + dcol) = z;
      lds_signal(flags + 5, l);
      if (w == 0) {
        lds_wait_ge(flags + 5, 8);
        LAS float* src = lds + L_ZP + ypg * YP_STR + 4 * yc4;
        f32x4 t = (*(LAS f32x4*)(src) + *(LAS f32x4*)(src + 4 * YP_STR)) + (*(LAS f32x4*)(src + 8 * YP_STR) + *(LAS f32x4*)(src + 12 * YP_STR));
#pragma unroll
        for (int r = 0; r < 4; ++r) { t[r] += dpp<XOR1>(t[r]); t[r] += dpp<XOR2>(t[r]); }
        zb = cvtpk(0.f, (ypg & 2) ? ((ypg & 1) ? t[3] : t[2]) : ((ypg & 1) ? t[1] : t[0]));
        asm volatile("" : "+v"(zb));
      }
    }
    if (DIAGACK && w > 0 && w < 14) { VMWAIT(0); TS(2); }
    TS(9);
    BAR();
    {
      const unsigned o0 = osrc[0], o1 = osrc[8];
      f32x4 y = mfma(mk & o0, WO[0], zero4);
      y = mfma(mk & o1, WO[1], y);
      if (valid) *(LAS f32x4*)ydst = y;
    }
    BAR();
    if (w == 0) {
      f32x4 t[8];
#pragma unroll
      for (int u = 0; u < 8; ++u) t[u] = *(LAS f32x4*)(ysrc + 4 * u * YP_STR);
      f32x4 a = ((t[0] + t[1]) + (t[2] + t[3])) + ((t[4] + t[5]) + (t[6] + t[7]));
#pragma unroll
      for (int r = 0; r < 4; ++r) { a[r] += dpp<XOR1>(a[r]); a[r] += dpp<XOR2>(a[r]); }
      const float ys = (ypg & 2) ? ((ypg & 1) ? a[3] : a[2]) : ((ypg & 1) ? a[1] : a[0]);
      put_gran2(ydg, (cvtpk(ys, ys) & 0xffffu) | zb, tlo);
      TS(10);
      if (DIAGACK) { VMWAIT(0); TS(3); }
    }
  }
  if (fin) {
    float sy, sz;
    if (FDELAY) __builtin_amdgcn_s_sleep(FDELAY);
    for (unsigned spins = 0;; ++spins) {
      u64 g[6];
#pragma unroll
      for (int u = 0; u < 6; ++u) g[u] = get_gran(ys + u * 4 * 64);
      SB();
      unsigned bad = 0;
      sy = 0.f; sz = 0.f;
#pragma unroll
      for (int u = 0; u < 6; ++u) {
        const unsigned v = (unsigned)(g[u] >> 32);
        bad |= (unsigned)g[u] ^ tlo;
        sy += bf2f(v & 0xffffu); sz += __uint_as_float(v & 0xffff0000u);
        asm volatile("" : "+v"(sy), "+v"(sz), "+v"(bad));
      }
      if (__all(bad == 0) || spins > SPIN_CAP) break;
      if (FSLEEP) __builtin_amdgcn_s_sleep(FSLEEP);
    }
    sy = xrow_sum(sy); sz = xrow_sum(sz);
    const float bb = (l & 1) ? __uint_as_float(b2fin & 0xffff0000u) : bf2f(b2fin & 0xffffu);
    const float xx = (l & 1) ? __uint_as_float(xfin & 0xffff0000u) : bf2f(xfin & 0xffffu);
    const float o2 = rbf(rbf(sz) + bb);
    const float gt = rbf(o2 * __builtin_amdgcn_rcpf(1.0f + __builtin_amdgcn_exp2f(-o2 * LOG2E)));
    const float res = xx + rbf(rbf(sy) * gt);
    const unsigned pk = cvtpk(res, dpp<XOR1>(res));
    if (l < 16 && !(l & 1)) ((GAS unsigned*)p.out)[8 * fq + (l >> 1)] = pk;
    if (fb == 0 && l == 0) __hip_atomic_store(ws + G_EPOCH, tag, RLX_AGENT);
    TS(11);
  }
}

static inline size_t ws_offset(size_t) { return 0; }

extern "C" void kernel_launch(void* const* d_in, const int* in_sizes, int n_in,
                              void* d_out, int out_size, void* d_ws, size_t ws_size,
                              hipStream_t stream) {
  Params p;
  p.x = (const unsigned short*)d_in[0];
  p.k = (const unsigned short*)d_in[1];
  p.v = (const unsigned short*)d_in[2];
  p.wq = (const unsigned short*)d_in[3];
  p.wo = (const unsigned short*)d_in[4];
  p.w1 = (const unsigned short*)d_in[6];
  p.b1 = (const unsigned short*)d_in[7];
  p.w2 = (const unsigned short*)d_in[8];
  p.b2 = (const unsigned short*)d_in[9];
  p.out = (unsigned short*)d_out;
  p.ws = (u64*)((char*)d_ws + ws_offset(ws_size));
  hipLaunchKernelGGL(fused_decode, dim3(NWG), dim3(NT), 0, stream, p);
}
```

```cpp
#include <hip/hip_runtime.h>
#include <hip/hip_bf16.h>
#include <stdint.h>

typedef unsigned v4u __attribute__((ext_vector_type(4)));
typedef unsigned v2u __attribute__((ext_vector_type(2)));
typedef float f32x4 __attribute__((ext_vector_type(4)));
typedef short bf16x8 __attribute__((ext_vector_type(8)));
typedef __bf16 bf16x2_t __attribute__((ext_vector_type(2)));
typedef unsigned long long u64;
#define GAS __attribute__((address_space(1)))
#define LAS __attribute__((address_space(3)))
#define RLX_AGENT __ATOMIC_RELAXED, __HIP_MEMORY_SCOPE_AGENT
#define RLX_WG __ATOMIC_RELAXED, __HIP_MEMORY_SCOPE_WORKGROUP
typedef GAS u64 gu64;

constexpr int DM = 640, HQ = 32, DH = 192, SK = 32768, WN = 4096, FF = 1536;
constexpr int NWG = 256, NT = 1024;
constexpr int ROWB = DH * 2;
constexpr int GRPB = 8 * ROWB;
constexpr float LOG2E = 1.4426950408889634f;
constexpr int RSRC_W3 = 0x00020000;
constexpr int AUX_NT = 2;
constexpr unsigned SPIN_CAP = 1u << 18;

constexpr int WIN = 2;
constexpr int EI = 9, WINE = 4;
constexpr int PUBPOS = 5;
constexpr int GI = 1;
constexpr int QI = 4;
constexpr int MK = 8, MG = 0;
constexpr int STEP_NS = 625;
constexpr int WO0_NS = 600, WO1_NS = 1500;

constexpr size_t G_EPOCH = 0;
constexpr size_t G_T = 64;
constexpr size_t G_Q = G_T + 256;
constexpr size_t G_O = G_Q + 256 * 64;
constexpr size_t G_Y = G_O + (size_t)NWG * 128;

constexpr int QP_STR = 68;
constexpr int OP_STR = 200;
constexpr int YP_STR = 80;
constexpr int L_QP = 0;
constexpr int L_OP = L_QP + 20 * QP_STR;
constexpr int L_YP = L_OP + 32 * OP_STR;
constexpr int L_ZP = L_YP + 32 * YP_STR;
constexpr int L_X = L_ZP + 16 * YP_STR;
constexpr int L_QB = L_X + 512;
constexpr int L_OB = L_QB + 96;
constexpr int L_HB = L_OB + 256;
constexpr int L_M = L_HB + 64;
constexpr int L_L = L_M + 16;
constexpr int L_T = L_L + 16;
constexpr int L_FLAG = L_T + 4;
constexpr int L_END = L_FLAG + 8;
enum { F_PROJ = 0, F_Q = 1, F_M = 2, F_VREQ = 3, F_L = 4, F_Z = 5, F_T = 6 };

struct Params {
  const unsigned short* x;
  const unsigned short* k;
  const unsigned short* v;
  const unsigned short* wq;
  const unsigned short* wo;
  const unsigned short* w1;
  const unsigned short* b1;
  const unsigned short* w2;
  const unsigned short* b2;
  unsigned short* out;
  u64* ws;
};

__device__ __forceinline__ float bf2f(unsigned h) { return __builtin_bit_cast(float, h << 16); }
__device__ __forceinline__ float bflo(unsigned v) { return __builtin_bit_cast(float, v << 16); }
__device__ __forceinline__ float bfhi(unsigned v) { return __builtin_bit_cast(float, v & 0xffff0000u); }
__device__ __forceinline__ unsigned cvtpk(float lo, float hi) {
  bf16x2_t r = {(__bf16)lo, (__bf16)hi};
  return __builtin_bit_cast(unsigned, r);
}
__device__ __forceinline__ float rbf(float f) { return bflo(cvtpk(f, f)); }
__device__ __forceinline__ float dot2(unsigned a, unsigned b, float acc) {
  return __builtin_amdgcn_fdot2_f32_bf16(__builtin_bit_cast(bf16x2_t, a), __builtin_bit_cast(bf16x2_t, b), acc, false);
}
template <int CTRL> __device__ __forceinline__ float dpp(float x) {
  return __builtin_bit_cast(float, __builtin_amdgcn_mov_dpp(__builtin_bit_cast(int, x), CTRL, 0xf, 0xf, true));
}
constexpr int XOR1 = 0xB1, XOR2 = 0x4E, HMIR = 0x141, ROR8 = 0x128;
__device__ __forceinline__ float sum4(float x) { x += dpp<XOR1>(x); x += dpp<XOR2>(x); return x; }
__device__ __forceinline__ float sum8(float x) { x = sum4(x); x += dpp<HMIR>(x); return x; }
__device__ __forceinline__ float sum16(float x) { x = sum8(x); x += dpp<ROR8>(x); return x; }
__device__ __forceinline__ float max16(float x) {
  x = fmaxf(x, dpp<XOR1>(x)); x = fmaxf(x, dpp<XOR2>(x)); x = fmaxf(x, dpp<HMIR>(x)); x = fmaxf(x, dpp<ROR8>(x)); return x; }
__device__ __forceinline__ float xrow_sum(float x) {
  auto s = __builtin_amdgcn_permlane16_swap(__float_as_uint(x), __float_as_uint(x), false, false);
  x = __uint_as_float(s[0]) + __uint_as_float(s[1]);
  auto t = __builtin_amdgcn_permlane32_swap(__float_as_uint(x), __float_as_uint(x), false, false);
  return __uint_as_float(t[0]) + __uint_as_float(t[1]);
}
__device__ __forceinline__ float xrow_max(float x) {
  auto s = __builtin_amdgcn_permlane16_swap(__float_as_uint(x), __float_as_uint(x), false, false);
  x = fmaxf(__uint_as_float(s[0]), __uint_as_float(s[1]));
  auto t = __builtin_amdgcn_permlane32_swap(__float_as_uint(x), __float_as_uint(x), false, false);
  return fmaxf(__uint_as_float(t[0]), __uint_as_float(t[1]));
}
__device__ __forceinline__ float pick4(f32x4 t, int i) { return (i & 2) ? ((i & 1) ? t[3] : t[2]) : ((i & 1) ? t[1] : t[0]); }
__device__ __forceinline__ f32x4 mfma(v4u a, v4u b, f32x4 c) {
  return __builtin_amdgcn_mfma_f32_16x16x32_bf16(__builtin_bit_cast(bf16x8, a), __builtin_bit_cast(bf16x8, b), c, 0, 0, 0);
}
__device__ __forceinline__ __amdgpu_buffer_rsrc_t mkrsrc(const void* p, unsigned bytes) {
  return __builtin_amdgcn_make_buffer_rsrc((void*)p, 0, bytes, RSRC_W3);
}
#define SB() __builtin_amdgcn_sched_barrier(0)
#define VMWAIT(n) asm volatile("s_waitcnt vmcnt(%0)" ::"n"(n) : "memory")
#define LGKM0() asm volatile("s_waitcnt lgkmcnt(0)" ::: "memory")
#define BAR() do { LGKM0(); __builtin_amdgcn_s_barrier(); asm volatile("" ::: "memory"); } while (0)

__device__ __forceinline__ void put_gran(gu64* g, float v, unsigned tlo, unsigned thi) {
  const unsigned hi = (__float_as_uint(v) & 0xffffff00u) | thi;
  __hip_atomic_store(g, ((u64)hi << 32) | tlo, RLX_AGENT);
}
__device__ __forceinline__ bool gran_ok(u64 g, unsigned tlo, unsigned thi) {
  return ((unsigned)g == tlo) & (((unsigned)(g >> 32) & 0xffu) == thi);
}
__device__ __forceinline__ float gran_val(u64 g) { return __uint_as_float((unsigned)(g >> 32) & 0xffffff00u); }
__device__ __forceinline__ void put_gran2(gu64* g, unsigned v, unsigned tlo) { __hip_atomic_store(g, ((u64)v << 32) | tlo, RLX_AGENT); }
__device__ __forceinline__ u64 get_gran(gu64* g) { return __hip_atomic_load(g, RLX_AGENT); }

__device__ __forceinline__ void lds_wait_ge(LAS unsigned* f, unsigned n) {
  while (__hip_atomic_load(f, RLX_WG) < n) __builtin_amdgcn_s_sleep(1);
  asm volatile("" ::: "memory");
}
__device__ __forceinline__ int opaque0() { int z; asm("v_mov_b32 %0, 0" : "=v"(z)); return z; }
__device__ __forceinline__ void lds_signal(LAS unsigned* f, int lane) {
  LGKM0();
  if (lane == 0) (void)__hip_atomic_fetch_add(f + opaque0(), 1u, RLX_WG);
}
__device__ __forceinline__ float qk_score(v4u k0, v4u k1, v4u k2, const v4u (&qf)[3]) {
  float d0 = dot2(k0.x, qf[0].x, 0.f), d1 = dot2(k0.y, qf[0].y, 0.f);
  d0 = dot2(k0.z, qf[0].z, d0); d1 = dot2(k0.w, qf[0].w, d1);
  d0 = dot2(k1.x, qf[1].x, d0); d1 = dot2(k1.y, qf[1].y, d1);
  d0 = dot2(k1.z, qf[1].z, d0); d1 = dot2(k1.w, qf[1].w, d1);
  d0 = dot2(k2.x, qf[2].x, d0); d1 = dot2(k2.y, qf[2].y, d1);
  d0 = dot2(k2.z, qf[2].z, d0); d1 = dot2(k2.w, qf[2].w, d1);
  return rbf(rbf(sum8(d0 + d1)) * 0.07216878364870322f);
}
__device__ __forceinline__ unsigned now10ns() { return (unsigned)__builtin_amdgcn_s_memrealtime(); }
__device__ __forceinline__ void pace(unsigned anchor, int ns) {
  while ((int)(now10ns() - anchor) < ns / 10) __builtin_amdgcn_s_sleep(1);
}
#define KLOAD(i) __builtin_amdgcn_raw_buffer_load_b128(rk, voff + ((i) % 3) * 128, woff + ((i) / 3) * GRPB, AUX_NT)
#define VLOAD(i) __builtin_amdgcn_raw_buffer_load_b128(rv, voff + ((i) % 3) * 128, woff + ((i) / 3) * GRPB, AUX_NT)

__global__ void __launch_bounds__(NT, 4) fused_decode(Params p) {
  __shared__ __attribute__((aligned(16))) float lds_[L_END];
  LAS float* lds = (LAS float*)lds_;
  LAS unsigned* flags = (LAS unsigned*)(lds + L_FLAG);
  const int tid = threadIdx.x, l = tid & 63, w = __builtin_amdgcn_readfirstlane(tid >> 6);
  const int b = blockIdx.x;
  const int s = b & 7, h = b >> 3;
  const bool outp = b >= 16;
  const int rb = outp ? (b - 16) / 10 : 0, cb = outp ? (b - 16) % 10 : 0;
  gu64* ws = (gu64*)p.ws;

  const unsigned voff = (unsigned)((l >> 3) * ROWB + (l & 7) * 16);
  const size_t kvoff = ((size_t)h * SK + (SK - WN) + (size_t)s * (WN / 8)) * DH;
  const __amdgpu_buffer_rsrc_t rk = mkrsrc(p.k + kvoff, (WN / 8) * ROWB);
  const __amdgpu_buffer_rsrc_t rv = mkrsrc(p.v + kvoff, (WN / 8) * ROWB);
  const int woff = w * (4 * GRPB);

  const bool proj = w < 10 && b < 240;
  const int row0 = (b & 1) * 320 + 32 * w;
  v4u K[12], V[12], A[4], X0, X1;
  if (__builtin_expect(proj, 1)) {
    const int ld = b < 192 ? HQ * DH : FF;
    const unsigned short* wb = b < 192 ? p.wq + (b >> 1) * 64 : p.w1 + ((b - 192) >> 1) * 64;
    const __amdgpu_buffer_rsrc_t ra = mkrsrc(wb + (size_t)row0 * ld, 32 * ld * 2);
    const __amdgpu_buffer_rsrc_t rx = mkrsrc(p.x, DM * 2);
    const unsigned aoff = (unsigned)((l >> 3) * ld * 2 + (l & 7) * 16);
    X0 = __builtin_amdgcn_raw_buffer_load_b128(rx, l * 16, 0, 0);
    X1 = __builtin_amdgcn_raw_buffer_load_b128(rx, l * 16 + 1024, 0, 0);
#pragma unroll
    for (int i = 0; i < 4; ++i) A[i] = __builtin_amdgcn_raw_buffer_load_b128(ra, aoff, i * 8 * ld * 2, AUX_NT);
  }
  SB();
#pragma unroll
  for (int i = 0; i < WIN; ++i) K[i] = KLOAD(i);
  SB();
  if (tid < 8) flags[tid] = 0;
  BAR();
  const u64 epv = get_gran(ws + G_EPOCH);
  SB();

  v4u mk;
  {
    const unsigned hm = (l & 1) ? 0xffff0000u : 0x0000ffffu;
    const int q4 = (l & 7) >> 1;
    mk.x = q4 == 0 ? hm : 0u; mk.y = q4 == 1 ? hm : 0u; mk.z = q4 == 2 ? hm : 0u; mk.w = q4 == 3 ? hm : 0u;
  }
  const bool valid = ((l >> 3) & 1) == (l >> 5);
  const int dcol = 8 * (l & 7) + 4 * ((l >> 4) & 1);
  const f32x4 zero4 = {0.f, 0.f, 0.f, 0.f};

  if (proj) {
    *(LAS v4u*)(lds + L_X + 4 * l) = X0;
    *(LAS v4u*)(lds + L_X + 256 + 4 * l) = X1;
    unsigned xr[4];
#pragma unroll
    for (int i = 0; i < 4; ++i) xr[i] = ((LAS unsigned short*)(lds + L_X))[row0 + 8 * i + (l >> 3)];
    float xs[16], sum = 0.f;
#pragma unroll
    for (int i = 0; i < 4; ++i) {
      xs[2 * i] = bflo(X0[i]); xs[2 * i + 1] = bfhi(X0[i]);
      xs[8 + 2 * i] = bflo(X1[i]); xs[8 + 2 * i + 1] = bfhi(X1[i]);
    }
#pragma unroll
    for (int i = 0; i < 8; ++i) sum += xs[i] + xs[8 + i];
    sum = xrow_sum(sum16(sum));
    const float mean = sum * (1.0f / DM);
    float sq = 0.f, sq1 = 0.f;
#pragma unroll
    for (int i = 0; i < 8; ++i) { const float d = xs[i] - mean, e = xs[8 + i] - mean; sq = fmaf(d, d, sq); sq1 = fmaf(e, e, sq1); }
    sq += l < 16 ? sq1 : 0.f;
    sq = xrow_sum(sum16(sq));
    const float sd = rbf(sqrtf(rbf(rbf(sq * (1.0f / DM)) + 1e-5f)));
    const float mb = rbf(mean);
    f32x4 d = zero4;
#pragma unroll
    for (int i = 0; i < 4; ++i) {
      const float xn = rbf(bf2f(xr[i]) - mb) / sd;
      d = mfma(mk & cvtpk(xn, xn), A[i], d);
    }
    if (valid) *(LAS f32x4*)(lds + L_QP + (2 * w + (l >> 5)) * QP_STR + dcol) = d;
    lds_signal(flags + F_PROJ, l);
  }

  u64 tag;
#pragma unroll
  for (int i = WIN; i < 12; ++i) {
    if ((i == PUBPOS + 1 || i == PUBPOS + 2) && w == 10) VMWAIT(WIN);
    else VMWAIT(WIN - 1);
    K[i] = KLOAD(i);
    SB();
    if (i == MK && w == 11 && l == 0) put_gran2(ws + G_T + b, now10ns(), (unsigned)tag);
    if (i == PUBPOS) {
      tag = ((u64)__builtin_amdgcn_readfirstlane((unsigned)(epv >> 32)) << 32 | (u64)__builtin_amdgcn_readfirstlane((unsigned)epv)) + 1;
      if (w == 10 && b < 240) {
        lds_wait_ge(flags + F_PROJ, 10);
        float acc = 0.f;
#pragma unroll
        for (int u = 0; u < 20; ++u) acc += lds[L_QP + u * QP_STR + l];
        put_gran(ws + G_Q + b * 64 + l, acc, (unsigned)tag, (unsigned)(tag >> 32) & 0xffu);
      }
      SB();
    }
  }
  static_assert(MK > PUBPOS, "the mark needs the tag");
  const unsigned tlo = (unsigned)tag, thi = (unsigned)(tag >> 32) & 0xffu;

  u64 g0 = 0, g1 = 0;
  unsigned b1v = 0;
  gu64* qsrc = ws + G_Q + (w < 15 ? 3 * h + (w - 12) : 96 + rb) * 128 + l;
  if (w >= 12) {
    g0 = get_gran(qsrc); g1 = get_gran(qsrc + 64);
    b1v = ((const GAS unsigned*)p.b1)[rb * 32 + (l >> 1)];
  }
  SB();

  float sc[4], mwg;
  v4u qf[3], sel[4];
  f32x4 acc[3] = {zero4, zero4, zero4};
  const int ha = (128 * rb) / 96;
  gu64* mlp = ws + G_O + (size_t)((ha + ((l >> 4) & 1)) * 8 + ((l >> 1) & 7)) * 128 + 96 + (l & 1);
  u64 gm = 0, gt = 0;
  unsigned anchor = 0;
#pragma unroll
  for (int i = 0; i < 12; ++i) {
    if (i >= EI) VMWAIT(WINE - 1);
    else if (i < WIN && w >= 12) VMWAIT(WIN + 2);
    else VMWAIT(WIN - 1);
    if (i == MG + 3) {
      lds_wait_ge(flags + F_T, 1);
      anchor = __builtin_amdgcn_readfirstlane(((LAS unsigned*)lds)[L_T]);
    }
    if (i >= MG + 3) pace(anchor, STEP_NS * (12 + i - MK));
    V[i] = VLOAD(i);
    SB();
    if (i == 11 && w >= 14) gm = get_gran(mlp);
    if (i == MG && w == 9) gt = get_gran(ws + G_T + (b & ~63) + l);
    if (i == MG + 2 && w == 9) {
      const unsigned t0 = now10ns();
      const bool ok = (unsigned)gt == tlo;
      const int n = __builtin_popcountll(__builtin_amdgcn_ballot_w64(ok));
      const int dt = (int)(xrow_sum(sum16(ok ? (float)(int)((unsigned)(gt >> 32) - t0) : 0.f)) * __builtin_amdgcn_rcpf((float)n));
      const bool use = n >= 16 && dt <= 0;
      if (l == 0) ((LAS unsigned*)lds)[L_T] = t0 + (unsigned)(use ? dt : -0x1000000);
      lds_signal(flags + F_T, l);
    }
    if (i == GI && w >= 12) {
      if (__builtin_expect(!__all(gran_ok(g0, tlo, thi) & gran_ok(g1, tlo, thi)), 0)) {
#pragma clang loop unroll(disable)
        for (unsigned spins = 0; spins < SPIN_CAP; ++spins) {
          __builtin_amdgcn_s_sleep(4);
          g0 = get_gran(qsrc); g1 = get_gran(qsrc + 64);
          if (__all(gran_ok(g0, tlo, thi) & gran_ok(g1, tlo, thi))) break;
        }
      }
      const float tot = gran_val(g0) + gran_val(g1);
      if (w < 15) {
        ((LAS unsigned short*)(lds + L_QB))[(w - 12) * 64 + l] = (unsigned short)(cvtpk(tot, tot) & 0xffffu);
      } else {
        float hv = rbf(rbf(tot) + ((l & 1) ? bfhi(b1v) : bflo(b1v)));
        hv = hv > 0.f ? hv : 0.f;
        hv = hv * hv;
        ((LAS unsigned*)(lds + L_HB))[l] = cvtpk(hv, hv);
      }
      lds_signal(flags + F_Q, l);
    }
    if (i == QI) {
      lds_wait_ge(flags + F_Q, 4);
#pragma unroll
      for (int j = 0; j < 3; ++j) qf[j] = *(LAS v4u*)(lds + L_QB + 32 * j + 4 * (l & 7));
      sc[0] = qk_score(K[0], K[1], K[2], qf);
      sc[1] = qk_score(K[3], K[4], K[5], qf);
    }
    if (i == QI + 1) {
      sc[2] = qk_score(K[6], K[7], K[8], qf);
      sc[3] = qk_score(K[9], K[10], K[11], qf);
      float mw = fmaxf(fmaxf(sc[0], sc[1]), fmaxf(sc[2], sc[3]));
      mw = xrow_max(fmaxf(mw, dpp<ROR8>(mw)));
      if (l == 0) lds[L_M + w] = mw;
      lds_signal(flags + F_M, l);
    }
    if (i == QI + 3) {
      lds_wait_ge(flags + F_M, 16);
      mwg = max16(lds[L_M + (l & 15)]);
      float lw = 0.f;
#pragma unroll
      for (int g = 0; g < 4; ++g) {
        const float pr = __builtin_amdgcn_exp2f((sc[g] - mwg) * LOG2E);
        lw += pr;
        sel[g] = mk & cvtpk(pr, pr);
      }
      lw = xrow_sum(lw + dpp<ROR8>(lw));
      if (l == 0) lds[L_L + w] = lw;
      lds_signal(flags + F_L, l);
#pragma unroll
      for (int u = 0; u < 6; ++u) acc[u % 3] = mfma(sel[u / 3], V[u], acc[u % 3]);
    }
    if (i == 10) {
#pragma unroll
      for (int u = 6; u < 9; ++u) acc[u % 3] = mfma(sel[2], V[u], acc[u % 3]);
    }
    SB();
    if (i == QI + 5 && w == 11) {
      lds_wait_ge(flags + F_L, 16);
      const float lsum = sum16(lds[L_L + (l & 15)]);
      if (l < 16) put_gran(ws + G_O + (size_t)b * 128 + 96 + l, l == 0 ? mwg : lsum, tlo, thi);
      SB();
    }
  }
  const unsigned oo = (unsigned)((l >> 3) * (DM * 2) + (l & 7) * 16);
  const __amdgpu_buffer_rsrc_t ro = mkrsrc(p.wo + (size_t)(256 * rb + 16 * w) * DM + 64 * cb, outp ? 16 * DM * 2 : 0);
  const __amdgpu_buffer_rsrc_t r2 = mkrsrc(p.w2 + (size_t)(64 * rb + 8 * (w & 7)) * DM + 64 * cb, outp && w < 8 ? 8 * DM * 2 : 0);
  pace(anchor, STEP_NS * (23 - MK) + WO0_NS);
  lds_signal(flags + F_VREQ, l);
  lds_wait_ge(flags + F_VREQ, 16);
  const v4u W2 = __builtin_amdgcn_raw_buffer_load_b128(r2, oo, 0, AUX_NT);
  const v4u WO0 = __builtin_amdgcn_raw_buffer_load_b128(ro, oo, 0, AUX_NT);
  SB();
#pragma unroll
  for (int u = 9; u < 12; ++u) acc[u % 3] = mfma(sel[3], V[u], acc[u % 3]);
  {
    LAS float* op = lds + L_OP + (w + 16 * (l >> 5)) * OP_STR + dcol;
#pragma unroll
    for (int j = 0; j < 3; ++j) if (valid) *(LAS f32x4*)(op + 64 * j) = acc[j];
  }
  BAR();
  if (w < 6) {
    const int c4 = tid >> 3, pg = tid & 7;
    LAS float* src = lds + L_OP + pg * OP_STR + 4 * c4;
    f32x4 t = (*(LAS f32x4*)(src) + *(LAS f32x4*)(src + 8 * OP_STR)) + (*(LAS f32x4*)(src + 16 * OP_STR) + *(LAS f32x4*)(src + 24 * OP_STR));
#pragma unroll
    for (int r = 0; r < 4; ++r) t[r] = sum8(t[r]);
    const unsigned pk = (pg & 1) ? cvtpk(t[2], t[3]) : cvtpk(t[0], t[1]);
    if (pg < 2) put_gran2(ws + G_O + (size_t)b * 128 + 2 * c4 + pg, pk, tlo);
  }
  pace(anchor, STEP_NS * (23 - MK) + WO1_NS);
  const v4u WO1 = __builtin_amdgcn_raw_buffer_load_b128(ro, oo, 8 * DM * 2, AUX_NT);
  SB();
  const int fq = (b - 16) / 5;
  const bool fin = w == 10 && outp && fq < 40 && (b - 16) % 5 == 0;
  unsigned xfin = 0, b2fin = 0;
  if (fin) {
    xfin = ((const GAS unsigned*)p.x)[8 * fq + ((l & 15) >> 1)];
    b2fin = ((const GAS unsigned*)p.b2)[8 * fq + ((l & 15) >> 1)];
  }
  if (!outp) return;

  gu64* ys = ws + G_Y + (size_t)(((fq >> 2) & 15) * 24 + (l >> 4)) * 64 + 16 * (fq & 3) + (l & 15);
  const int yc4 = l >> 2, ypg = l & 3;
  LAS unsigned* osrc = (LAS unsigned*)(lds + L_OB) + 16 * w + (l >> 3);
  LAS float* ydst = lds + L_YP + (2 * w + (l >> 5)) * YP_STR + dcol;
  LAS float* ysrc = lds + L_YP + ypg * YP_STR + 4 * yc4;
  gu64* ydg = ws + G_Y + (size_t)(cb * 24 + rb) * 64 + l;
  asm volatile("" : "+v"(ys), "+v"(osrc), "+v"(ydst), "+v"(ysrc), "+v"(ydg));
  unsigned zb = 0;
  if (w >= 14) {
    const int d = (w & 1) * 64 + l, pr = 128 * rb + d;
    const int hh = pr / 96;
    gu64* og = ws + G_O + (size_t)(hh * 8) * 128 + (pr - hh * 96);
    LAS v2u* odst = (LAS v2u*)(lds + L_OB + 2 * d);
    asm volatile("" : "+v"(odst));
    if (!__all(gran_ok(gm, tlo, thi))) {
#pragma clang loop unroll(disable)
      for (unsigned spins = 0; spins < SPIN_CAP; ++spins) {
        gm = get_gran(mlp);
        if (__all(gran_ok(gm, tlo, thi))) break;
        __builtin_amdgcn_s_sleep(2);
      }
    }
    float fi[8];
    {
      const float ml = gran_val(gm);
      const float mo = dpp<XOR1>(ml);
      const float ms = (l & 1) ? mo : ml, ls = (l & 1) ? ml : mo;
      const float M = max16(ms);
      const float e = __builtin_amdgcn_exp2f((ms - M) * LOG2E);
      const float f = e * __builtin_amdgcn_rcpf(0.5f * sum16(e * ls));
#pragma unroll
      for (int i = 0; i < 8; ++i) {
        const float fa = __builtin_bit_cast(float, __builtin_amdgcn_readlane(__builtin_bit_cast(int, f), 2 * i));
        const float fb = __builtin_bit_cast(float, __builtin_amdgcn_readlane(__builtin_bit_cast(int, f), 16 + 2 * i));
        fi[i] = hh == ha ? fa : fb;
        asm volatile("" : "+v"(fi[i]));
      }
    }
    float o0, o1;
    for (unsigned spins = 0;; ++spins) {
      u64 g[8];
#pragma unroll
      for (int i = 0; i < 8; ++i) g[i] = get_gran(og + i * 128);
      SB();
      unsigned bad = 0;
      o0 = 0.f; o1 = 0.f;
#pragma unroll
      for (int i = 0; i < 8; ++i) {
        const unsigned v = (unsigned)(g[i] >> 32);
        bad |= (unsigned)g[i] ^ tlo;
        o0 = fmaf(fi[i], bflo(v), o0);
        o1 = fmaf(fi[i], bfhi(v), o1);
        asm volatile("" : "+v"(o0), "+v"(o1), "+v"(bad));
      }
      if (__all(bad == 0) || spins > SPIN_CAP) break;
    }
    *odst = v2u{cvtpk(o0, o0), cvtpk(o1, o1)};
  } else if (w < 8) {
    const f32x4 z = mfma(mk & ((LAS unsigned*)(lds + L_HB))[8 * w + (l >> 3)], W2, zero4);
    if (valid) *(LAS f32x4*)(lds + L_ZP + (2 * w + (l >> 5)) * YP_STR + dcol) = z;
    lds_signal(flags + F_Z, l);
    if (w == 0) {
      lds_wait_ge(flags + F_Z, 8);
      LAS float* src = lds + L_ZP + ypg * YP_STR + 4 * yc4;
      f32x4 t = (*(LAS f32x4*)(src) + *(LAS f32x4*)(src + 4 * YP_STR)) + (*(LAS f32x4*)(src + 8 * YP_STR) + *(LAS f32x4*)(src + 12 * YP_STR));
#pragma unroll
      for (int r = 0; r < 4; ++r) t[r] = sum4(t[r]);
      zb = cvtpk(0.f, pick4(t, ypg));
      asm volatile("" : "+v"(zb));
    }
  }
  BAR();
  {
    const unsigned o0 = osrc[0], o1 = osrc[8];
    f32x4 y = mfma(mk & o0, WO0, zero4);
    y = mfma(mk & o1, WO1, y);
    if (valid) *(LAS f32x4*)ydst = y;
  }
  BAR();
  if (w == 0) {
    f32x4 t[8];
#pragma unroll
    for (int u = 0; u < 8; ++u) t[u] = *(LAS f32x4*)(ysrc + 4 * u * YP_STR);
    f32x4 a = ((t[0] + t[1]) + (t[2] + t[3])) + ((t[4] + t[5]) + (t[6] + t[7]));
#pragma unroll
    for (int r = 0; r < 4; ++r) a[r] = sum4(a[r]);
    const float yv = pick4(a, ypg);
    put_gran2(ydg, (cvtpk(yv, yv) & 0xffffu) | zb, tlo);
  } else if (fin) {
    float sy, sz;
    for (unsigned spins = 0;; ++spins) {
      u64 g[6];
#pragma unroll
      for (int u = 0; u < 6; ++u) g[u] = get_gran(ys + u * 4 * 64);
      SB();
      unsigned bad = 0;
      sy = 0.f; sz = 0.f;
#pragma unroll
      for (int u = 0; u < 6; ++u) {
        const unsigned v = (unsigned)(g[u] >> 32);
        bad |= (unsigned)g[u] ^ tlo;
        sy += bflo(v); sz += bfhi(v);
        asm volatile("" : "+v"(sy), "+v"(sz), "+v"(bad));
      }
      if (__all(bad == 0) || spins > SPIN_CAP) break;
    }
    sy = xrow_sum(sy); sz = xrow_sum(sz);
    const float o2 = rbf(rbf(sz) + ((l & 1) ? bfhi(b2fin) : bflo(b2fin)));
    const float gate = rbf(o2 * __builtin_amdgcn_rcpf(1.0f + __builtin_amdgcn_exp2f(-o2 * LOG2E)));
    const float res = ((l & 1) ? bfhi(xfin) : bflo(xfin)) + rbf(rbf(sy) * gate);
    const unsigned pk = cvtpk(res, dpp<XOR1>(res));
    if (l < 16 && !(l & 1)) ((GAS unsigned*)p.out)[8 * fq + (l >> 1)] = pk;
    if (fq == 0 && l == 0) __hip_atomic_store(ws + G_EPOCH, tag, RLX_AGENT);
  }
}

extern "C" void kernel_launch(void* const* d_in, const int* in_sizes, int n_in,
                              void* d_out, int out_size, void* d_ws, size_t ws_size,
                              hipStream_t stream) {
  Params p;
  p.x = (const unsigned short*)d_in[0];
  p.k = (const unsigned short*)d_in[1];
  p.v = (const unsigned short*)d_in[2];
  p.wq = (const unsigned short*)d_in[3];
  p.wo = (const unsigned short*)d_in[4];
  p.w1 = (const unsigned short*)d_in[6];
  p.b1 = (const unsigned short*)d_in[7];
  p.w2 = (const unsigned short*)d_in[8];
  p.b2 = (const unsigned short*)d_in[9];
  p.out = (unsigned short*)d_out;
  p.ws = (u64*)d_ws;
  hipLaunchKernelGGL(fused_decode, dim3(NWG), dim3(NT), 0, stream, p);
}
```
